```python
import torch

def _softmax(x, axis=-1):
    return torch.softmax(x, dim=axis)
def _gelu(x):
    return torch.nn.functional.gelu(x, approximate="tanh")
def _silu(x):
    return torch.nn.functional.silu(x)
def _sigmoid(x):
    return torch.sigmoid(x)
def _layernorm(x, axis=-1, eps=1e-5):
    m = x.mean(dim=axis, keepdim=True)
    v = x.var(dim=axis, keepdim=True, unbiased=False)
    return (x - m) / torch.sqrt(v + eps)
def _scatter_add(tgt, idx, src, axis=0):
    return tgt.clone().index_add_(axis, idx, src)
def _segment_sum(src, seg, n):
    out = torch.zeros((n,) + src.shape[1:], dtype=src.dtype)
    return out.index_add_(0, seg, src)
def _pad(x, pw):
    flat = tuple(v for pair in reversed(pw) for v in pair)
    return torch.nn.functional.pad(x, flat)

kernel_name = 'standalone__layernorm_linear__sampling'

def setup_inputs():

    _w = (lambda a: (lambda t: t.to(torch.bfloat16) if t.is_floating_point() else t)(torch.as_tensor(a)))
    B, S, D = 1024, 4, 1024
    return {
        "x": _w((lambda *s: torch.randn(*s, dtype=torch.bfloat16) * 0.05)(B, S, D)),
        "g": _w((lambda *s: torch.randn(*s, dtype=torch.bfloat16) * 0.05)(D) + 1.0),
        "b": _w((lambda *s: torch.randn(*s, dtype=torch.bfloat16) * 0.4)(D)),
        "w": _w((lambda *s: torch.randn(*s, dtype=torch.bfloat16) * 0.05)(D, D)),
    }

def reference(x, g, b, w):
    return (_layernorm(x, axis=-1) * g + b) @ w
```

```cpp
#include <hip/hip_runtime.h>
#include <hip/hip_bf16.h>
using bf16 = __hip_bfloat16;
using bf16x8 = __attribute__((ext_vector_type(8))) short;
using f32x4 = __attribute__((ext_vector_type(4))) float;
typedef unsigned int u32;
__device__ __forceinline__ float bf2f(unsigned short v){ return __uint_as_float(((u32)v)<<16); }
__device__ __forceinline__ unsigned short f2bf(float f){ __hip_bfloat16 h = __float2bfloat16(f); return *reinterpret_cast<unsigned short*>(&h); }

__global__ __launch_bounds__(256) void ln_k(const unsigned short* x, const unsigned short* g, const unsigned short* b, unsigned short* y){
  int row = blockIdx.x*4 + (threadIdx.x>>6); int lane = threadIdx.x&63;
  const unsigned short* xr = x + (size_t)row*1024;
  bf16x8 v0 = *(const bf16x8*)(xr + lane*8), v1 = *(const bf16x8*)(xr + 512 + lane*8);
  float f[16]; float s=0;
  for(int j=0;j<8;++j){ f[j]=bf2f(v0[j]); f[8+j]=bf2f(v1[j]); }
  for(int j=0;j<16;++j) s+=f[j];
  for(int o=32;o;o>>=1) s+=__shfl_xor(s,o);
  float mu=s/1024.f; float q=0;
  for(int j=0;j<16;++j){ float d=f[j]-mu; q+=d*d; }
  for(int o=32;o;o>>=1) q+=__shfl_xor(q,o);
  float rs = rsqrtf(q/1024.f+1e-5f);
  bf16x8 g0=*(const bf16x8*)(g+lane*8), g1=*(const bf16x8*)(g+512+lane*8);
  bf16x8 b0=*(const bf16x8*)(b+lane*8), b1=*(const bf16x8*)(b+512+lane*8);
  bf16x8 o0,o1;
  for(int j=0;j<8;++j){ o0[j]=f2bf((f[j]-mu)*rs*bf2f(g0[j])+bf2f(b0[j])); o1[j]=f2bf((f[8+j]-mu)*rs*bf2f(g1[j])+bf2f(b1[j])); }
  *(bf16x8*)(y+(size_t)row*1024+lane*8)=o0; *(bf16x8*)(y+(size_t)row*1024+512+lane*8)=o1;
}
__global__ __launch_bounds__(256) void gemm_k(const unsigned short* y, const unsigned short* w, unsigned short* out){
  __shared__ unsigned short sA[64][40]; __shared__ unsigned short sB[64][40];
  int bm=blockIdx.y*64, bn=blockIdx.x*64; int tid=threadIdx.x, wid=tid>>6, lane=tid&63;
  int wr=wid>>1, wc=wid&1, fr=lane&15, fq=lane>>4;
  f32x4 acc[2][2]={};
  for(int k0=0;k0<1024;k0+=32){
    { int r=tid>>2, c=(tid&3)*8; *(bf16x8*)&sA[r][c] = *(const bf16x8*)(y+(size_t)(bm+r)*1024+k0+c); }
    { int k=tid>>3, c=(tid&7)*8; bf16x8 v=*(const bf16x8*)(w+(size_t)(k0+k)*1024+bn+c); for(int j=0;j<8;++j) sB[c+j][k]=v[j]; }
    __syncthreads();
    bf16x8 a[2],bb[2];
    for(int m=0;m<2;++m) a[m]=*(const bf16x8*)&sA[wr*32+m*16+fr][fq*8];
    for(int n=0;n<2;++n) bb[n]=*(const bf16x8*)&sB[wc*32+n*16+fr][fq*8];
    for(int m=0;m<2;++m)for(int n=0;n<2;++n) acc[m][n]=__builtin_amdgcn_mfma_f32_16x16x32_bf16(a[m],bb[n],acc[m][n],0,0,0);
    __syncthreads();
  }
  for(int m=0;m<2;++m)for(int n=0;n<2;++n)for(int j=0;j<4;++j)
    out[(size_t)(bm+wr*32+m*16+fq*4+j)*1024+bn+wc*32+n*16+fr]=f2bf(acc[m][n][j]);
}
extern "C" void kernel_launch(void* const* d_in, const int* in_sizes, int n_in,
                              void* d_out, int out_size, void* d_ws, size_t ws_size, hipStream_t stream) {
  const unsigned short* x=(const unsigned short*)d_in[0]; const unsigned short* g=(const unsigned short*)d_in[1];
  const unsigned short* b=(const unsigned short*)d_in[2]; const unsigned short* w=(const unsigned short*)d_in[3];
  unsigned short* y=(unsigned short*)d_ws;
  ln_k<<<1024,256,0,stream>>>(x,g,b,y);
  gemm_k<<<dim3(16,64),256,0,stream>>>(y,w,(unsigned short*)d_out);
}
```

```cpp
#include <hip/hip_runtime.h>
#include <hip/hip_bf16.h>
typedef unsigned int u32;
typedef unsigned short u16;
using bf16x8 = __attribute__((ext_vector_type(8))) short;
using s16x4  = __attribute__((ext_vector_type(4))) short;
using f32x4  = __attribute__((ext_vector_type(4))) float;
using f32x16 = __attribute__((ext_vector_type(16))) float;
using u32x4  = __attribute__((ext_vector_type(4))) u32;
using u32x2  = __attribute__((ext_vector_type(2))) u32;
typedef __attribute__((address_space(3))) s16x4* lds_s16x4_p;

#define ROTMODE 0

constexpr int KD = 1024, ND = 1024;
constexpr int SA = 2, SB = 5;
constexpr int OFF_A = 0;
constexpr int OFF_B = OFF_A + SA * 16384;
constexpr int OFF_G32 = OFF_B + SB * 16384;
constexpr int OFF_G16 = OFF_G32 + 4096;
constexpr int OFF_B16 = OFF_G16 + 2048 + 64;
constexpr int OFF_Z16 = OFF_B16 + 2048 + 64;
constexpr int OFF_ST  = OFF_Z16 + 2048;
constexpr int OFF_C   = OFF_ST + 1024;
constexpr int LDS_BYTES = OFF_C + 1024;

template<int N> __device__ __forceinline__ void wait_vm() { asm volatile("s_waitcnt vmcnt(%0)" :: "i"(N) : "memory"); }
__device__ __forceinline__ void wait_lgkm0() { asm volatile("s_waitcnt lgkmcnt(0)" ::: "memory"); }
__device__ __forceinline__ void bar() { __builtin_amdgcn_s_barrier(); }
__device__ __forceinline__ u32 cvt_pk(float lo, float hi) {
  typedef __attribute__((ext_vector_type(2))) float f2; typedef __attribute__((ext_vector_type(2))) __bf16 b2;
  f2 v = {lo, hi}; b2 r = __builtin_convertvector(v, b2); return __builtin_bit_cast(u32, r);
}

__global__ __launch_bounds__(512, 2)
void lnlin_kernel(const u16* __restrict__ x, const u16* __restrict__ g, const u16* __restrict__ b,
                  const u16* __restrict__ w, u16* __restrict__ out) {
  extern __shared__ __attribute__((aligned(16))) char smem[];
  const int bid = blockIdx.x, xcd = bid & 7, idx = bid >> 3;
  const int rp = xcd * 4 + (idx >> 3), cp = idx & 7;
  const int tid = threadIdx.x, lane = tid & 63;
  const int wid = __builtin_amdgcn_readfirstlane(tid >> 6);
  const int rot = 0;

  if (wid < 4) {
    const int wm = wid >> 1, wn = wid & 1;
    const int dr = lane >> 4;
    const u32 wvoff = (u32)(((wid * 16 + dr) * ND + cp * 128 + 8 * ((lane & 15) ^ (dr << 2))) * 2);
    u32x4 wrsrc;
    wrsrc[0] = (u32)(size_t)w; wrsrc[1] = (u32)((size_t)w >> 32) & 0xffffu; wrsrc[2] = KD * ND * 2; wrsrc[3] = 0x00020000u;
    const u32 lds0 = (u32)(size_t)smem;
    auto issueB = [&](int u) {
      const int kt = (u + rot) & 15;
#pragma unroll
      for (int i = 0; i < 4; ++i) {
        const u32 soff = (u32)(kt * 64 + i * 4) * (ND * 2);
        const u32 dst = lds0 + OFF_B + (u % SB) * 16384 + wid * 4096 + i * 1024;
        u32 keep;
        asm volatile("s_mov_b32 %0, m0\n\ts_mov_b32 m0, %4\n\ts_nop 0\n\t"
                     "buffer_load_dwordx4 %1, %2, %3 offen lds\n\ts_mov_b32 m0, %0"
                     : "=&s"(keep) : "v"(wvoff), "s"(wrsrc), "s"(soff), "s"(dst) : "memory");
      }
    };
#pragma unroll
    for (int u = 0; u < SB - 1; ++u) issueB(u);

    const int h = lane >> 5;
    const int xr = (lane >> 1) & 7;
    u32 xaddr[4];
#pragma unroll
    for (int s = 0; s < 4; ++s) xaddr[s] = OFF_A + (wm * 64 + (lane & 31)) * 128 + (((2 * s + h) ^ xr) * 16);
    const int blk = (lane >> 4) & 1, q = (lane & 15) >> 2, p4 = lane & 3;
    u32 waddr[2];
#pragma unroll
    for (int nt = 0; nt < 2; ++nt)
      waddr[nt] = OFF_B + (8 * h + q) * 256 + 16 * (4 * ((wn * 2 + (nt ^ wm)) ^ q) + 2 * blk + (p4 >> 1)) + 8 * (p4 & 1);
    const int jp = lane & 15, kg = lane >> 4;
    u32 seladdr = OFF_Z16;
    if ((jp & 1) == (kg & 1) && jp < 4) seladdr = (jp < 2 ? OFF_G16 : OFF_B16) + (kg >> 1) * 16;

    f32x16 acc[2][2] = {};
    f32x4 hacc = {0.f, 0.f, 0.f, 0.f};

    bar();
    wait_vm<(SB - 2) * 4>();
    bar();
#pragma unroll
    for (int u = 0; u < 16; ++u) {
      if (u >= 1 && u + SB - 1 < 16 + 0) issueB(u + SB - 1);
      else if (u == 0) issueB(SB - 1);
      const int kt = (u + rot) & 15;
      const char* sa = smem + (u % SA) * 16384;
      const char* sb = smem + (u % SB) * 16384;
      const char* sel = smem + seladdr + kt * 128;
      bf16x8 xf[2][2], wf[2][2], sf[2];
      auto ldfrag = [&](int s) {
#pragma unroll
        for (int nt = 0; nt < 2; ++nt) {
          s16x4 lo = __builtin_amdgcn_ds_read_tr16_b64_v4i16((lds_s16x4_p)(sb + waddr[nt] + s * 4096));
          s16x4 hi = __builtin_amdgcn_ds_read_tr16_b64_v4i16((lds_s16x4_p)(sb + waddr[nt] + s * 4096 + 1024));
          wf[s & 1][nt] = __builtin_shufflevector(lo, hi, 0, 1, 2, 3, 4, 5, 6, 7);
          xf[s & 1][nt] = *(const bf16x8*)(sa + xaddr[s] + nt * 4096);
        }
        sf[s & 1] = *(const bf16x8*)(sel + s * 32);
      };
      ldfrag(0);
#pragma unroll
      for (int s = 0; s < 4; ++s) {
        if (s < 3) ldfrag(s + 1);
        __builtin_amdgcn_sched_barrier(0);
#pragma unroll
        for (int mt = 0; mt < 2; ++mt)
#pragma unroll
          for (int nt = 0; nt < 2; ++nt)
            acc[mt][nt] = __builtin_amdgcn_mfma_f32_32x32x16_bf16(wf[s & 1][nt], xf[s & 1][mt], acc[mt][nt], 0, 0, 0);
        hacc = __builtin_amdgcn_mfma_f32_16x16x32_bf16(wf[s & 1][0], sf[s & 1], hacc, 0, 0, 0);
        __builtin_amdgcn_sched_barrier(0);
      }
      if (u < 15) {
        constexpr int dummy = 0; (void)dummy;
        const int last = (u + SB - 1 < 15) ? (u + SB - 1) : 15;
        const int nleft = last - (u + 1);
        if (nleft >= 3) wait_vm<12>(); else if (nleft == 2) wait_vm<8>(); else if (nleft == 1) wait_vm<4>(); else wait_vm<0>();
        wait_lgkm0();
        bar();
      }
    }
    if (jp < 4) *(f32x4*)(smem + OFF_C + (jp >> 1) * 512 + (wn * 64 + wm * 32 + (jp & 1) * 16 + 4 * kg) * 4) = hacc;
    wait_lgkm0();
    bar();
#pragma unroll
    for (int mt = 0; mt < 2; ++mt) {
      const int lr = wm * 64 + mt * 32 + (lane & 31);
      const float2 st = *(const float2*)(smem + OFF_ST + lr * 8);
      u16* orow = out + (size_t)(rp * 128 + lr) * ND + cp * 128 + wn * 64 + h * 8;
#pragma unroll
      for (int nt = 0; nt < 2; ++nt) {
        u32 d[4][2];
#pragma unroll
        for (int qq = 0; qq < 4; ++qq) {
          const int n0 = wn * 64 + (nt ^ wm) * 32 + 8 * qq + 4 * h;
          const f32x4 c1 = *(const f32x4*)(smem + OFF_C + n0 * 4);
          const f32x4 c2 = *(const f32x4*)(smem + OFF_C + 512 + n0 * 4);
          float o[4];
#pragma unroll
          for (int e = 0; e < 4; ++e) o[e] = acc[mt][nt][4 * qq + e] * st.x + (c1[e] * st.y + c2[e]);
          d[qq][0] = cvt_pk(o[0], o[1]); d[qq][1] = cvt_pk(o[2], o[3]);
        }
#pragma unroll
        for (int qq = 0; qq < 4; qq += 2) {
          auto r0 = __builtin_amdgcn_permlane32_swap(d[qq][0], d[qq + 1][0], false, false);
          auto r1 = __builtin_amdgcn_permlane32_swap(d[qq][1], d[qq + 1][1], false, false);
          u32x4 v = {r0[0], r1[0], r0[1], r1[1]};
          *(u32x4*)(orow + (nt ^ wm) * 32 + 8 * qq) = v;
        }
      }
    }
  } else {
    const int sw = wid - 4, st = tid - 256;
    const int c = lane & 7, rr = lane >> 3;
    u32x4 gv, bv;
    if (st < 128) gv = *(const u32x4*)(g + st * 8); else bv = *(const u32x4*)(b + (st - 128) * 8);
    const u16* xsrc = x + (size_t)(rp * 128 + sw * 32 + rr) * KD + c * 8;
    constexpr int DX = 3;
    u32x4 xv[DX][4];
    auto issueX = [&](int u) {
      const int kt = (u + rot) & 15;
#pragma unroll
      for (int i = 0; i < 4; ++i) xv[u % DX][i] = *(const u32x4*)(xsrc + (size_t)i * 8 * KD + kt * 64);
    };
#pragma unroll
    for (int u = 0; u < DX; ++u) issueX(u);
    if (st < 128) {
      *(u32x4*)(smem + OFF_G16 + st * 16) = gv;
      f32x4 lo, hi;
      lo[0] = __uint_as_float(gv[0] << 16); lo[1] = __uint_as_float(gv[0] & 0xffff0000u);
      lo[2] = __uint_as_float(gv[1] << 16); lo[3] = __uint_as_float(gv[1] & 0xffff0000u);
      hi[0] = __uint_as_float(gv[2] << 16); hi[1] = __uint_as_float(gv[2] & 0xffff0000u);
      hi[2] = __uint_as_float(gv[3] << 16); hi[3] = __uint_as_float(gv[3] & 0xffff0000u);
      *(f32x4*)(smem + OFF_G32 + st * 32) = lo; *(f32x4*)(smem + OFF_G32 + st * 32 + 16) = hi;
    } else {
      *(u32x4*)(smem + OFF_B16 + (st - 128) * 16) = bv;
      u32x4 z = {0, 0, 0, 0};
      *(u32x4*)(smem + OFF_Z16 + (st - 128) * 16) = z;
    }
    wait_lgkm0();
    bar();
    const u32 wbase = OFF_A + sw * 4096 + rr * 128 + ((c ^ (rr >> 1)) * 16);
    float s1[4] = {0.f, 0.f, 0.f, 0.f}, s2[4] = {0.f, 0.f, 0.f, 0.f};
    auto scale = [&](int u) {
      const int kt = (u + rot) & 15;
      const f32x4 g0 = *(const f32x4*)(smem + OFF_G32 + kt * 256 + c * 32);
      const f32x4 g1 = *(const f32x4*)(smem + OFF_G32 + kt * 256 + c * 32 + 16);
#pragma unroll
      for (int i = 0; i < 4; ++i) {
        const u32x4 v = xv[u % DX][i];
        u32x4 o;
        float t0, t1, t2, t3;
#define SC2(OA, OB, VA, VB, GA, GB, GC, GD) \
        "v_lshlrev_b32 %6, 16, " VA "\n\t" \
        "v_and_b32 %7, %22, " VA "\n\t" \
        "v_lshlrev_b32 %8, 16, " VB "\n\t" \
        "v_and_b32 %9, %22, " VB "\n\t" \
        "v_add_f32 %4, %4, %6\n\t" \
        "v_fmac_f32 %5, %6, %6\n\t" \
        "v_mul_f32 %6, " GA ", %6\n\t" \
        "v_add_f32 %4, %4, %7\n\t" \
        "v_fmac_f32 %5, %7, %7\n\t" \
        "v_mul_f32 %7, " GB ", %7\n\t" \
        "v_add_f32 %4, %4, %8\n\t" \
        "v_fmac_f32 %5, %8, %8\n\t" \
        "v_mul_f32 %8, " GC ", %8\n\t" \
        "v_add_f32 %4, %4, %9\n\t" \
        "v_fmac_f32 %5, %9, %9\n\t" \
        "v_mul_f32 %9, " GD ", %9\n\t" \
        "v_cvt_pk_bf16_f32 " OA ", %6, %7\n\t" \
        "v_cvt_pk_bf16_f32 " OB ", %8, %9\n\t"
        asm volatile(
          SC2("%0", "%1", "%10", "%11", "%14", "%15", "%16", "%17")
          SC2("%2", "%3", "%12", "%13", "%18", "%19", "%20", "%21")
          : "=&v"(o[0]), "=&v"(o[1]), "=&v"(o[2]), "=&v"(o[3]), "+v"(s1[i]), "+v"(s2[i]),
            "=&v"(t0), "=&v"(t1), "=&v"(t2), "=&v"(t3)
          : "v"(v[0]), "v"(v[1]), "v"(v[2]), "v"(v[3]),
            "v"(g0[0]), "v"(g0[1]), "v"(g0[2]), "v"(g0[3]), "v"(g1[0]), "v"(g1[1]), "v"(g1[2]), "v"(g1[3]),
            "s"(0xffff0000u));
        *(u32x4*)(smem + (u % SA) * 16384 + ((wbase ^ ((i & 1) * 64)) + i * 1024)) = o;
      }
    };
    scale(0);
    issueX(DX);
    wait_lgkm0();
    bar();
#pragma unroll
    for (int u = 0; u < 15; ++u) {
      scale(u + 1);
      if (u + 1 + DX < 16) issueX(u + 1 + DX);
      wait_lgkm0();
      bar();
    }
#pragma unroll
    for (int i = 0; i < 4; ++i) {
#pragma unroll
      for (int o = 1; o < 8; o <<= 1) { s1[i] += __shfl_xor(s1[i], o); s2[i] += __shfl_xor(s2[i], o); }
      const float mu = s1[i] * (1.f / KD);
      const float var = fmaxf(s2[i] * (1.f / KD) - mu * mu, 0.f);
      const float rs = rsqrtf(var + 1e-5f);
      if (c == 0) *(float2*)(smem + OFF_ST + (sw * 32 + i * 8 + rr) * 8) = make_float2(rs, -mu * rs);
    }
    wait_lgkm0();
    bar();
  }
}

extern "C" void kernel_launch(void* const* d_in, const int* in_sizes, int n_in,
                              void* d_out, int out_size, void* d_ws, size_t ws_size, hipStream_t stream) {
  const u16* x = (const u16*)d_in[0]; const u16* g = (const u16*)d_in[1];
  const u16* b = (const u16*)d_in[2]; const u16* w = (const u16*)d_in[3];
  (void)hipFuncSetAttribute((const void*)lnlin_kernel, hipFuncAttributeMaxDynamicSharedMemorySize, LDS_BYTES);
  lnlin_kernel<<<256, 512, LDS_BYTES, stream>>>(x, g, b, w, (u16*)d_out);
}
```

```cpp
#define REP 1
#include <hip/hip_runtime.h>
#include <hip/hip_bf16.h>
typedef unsigned int u32;
typedef unsigned short u16;
using bf16x8 = __attribute__((ext_vector_type(8))) short;
using s16x4  = __attribute__((ext_vector_type(4))) short;
using f32x4  = __attribute__((ext_vector_type(4))) float;
using f32x16 = __attribute__((ext_vector_type(16))) float;
using u32x4  = __attribute__((ext_vector_type(4))) u32;
using u32x2  = __attribute__((ext_vector_type(2))) u32;
typedef __attribute__((address_space(3))) s16x4* lds_s16x4_p;

#define ROTMODE 6

constexpr int MD = 4096, KD = 1024, ND = 1024;
constexpr int SA = 2, SB = 4;
constexpr int OFF_B = 0;
constexpr int OFF_A = OFF_B + SB * 16384;
constexpr int OFF_G32 = OFF_A + SA * 16384;
constexpr int OFF_G16 = OFF_G32 + 16384;
constexpr int OFF_B16 = OFF_G16 + 2048 + 64;
constexpr int OFF_Z16 = OFF_B16 + 2048 + 64;
constexpr int OFF_ST  = OFF_Z16 + 2048;
constexpr int OFF_C   = OFF_ST + 1024;
constexpr int OFF_STAMP = OFF_C + 1024;
constexpr int LDS_BYTES = OFF_STAMP + 512;
constexpr u32 OOB = 0x40000000u;

__device__ __forceinline__ void wait_lgkm0();
#define ST(i)
#define STDUMP()
template<int N> __device__ __forceinline__ void wait_vm() { asm volatile("s_waitcnt vmcnt(%0)" :: "i"(N) : "memory"); }
__device__ __forceinline__ void wait_lgkm0() { asm volatile("s_waitcnt lgkmcnt(0)" ::: "memory"); }
__device__ __forceinline__ void bar() { __builtin_amdgcn_s_barrier(); }
__device__ __forceinline__ u32 cvt_pk(float lo, float hi) {
  typedef __attribute__((ext_vector_type(2))) float f2; typedef __attribute__((ext_vector_type(2))) __bf16 b2;
  f2 v = {lo, hi}; b2 r = __builtin_convertvector(v, b2); return __builtin_bit_cast(u32, r);
}
__device__ __forceinline__ u32x4 mk_rsrc(const void* p, u32 bytes) {
  u32x4 r; r[0] = (u32)(size_t)p; r[1] = (u32)((size_t)p >> 32) & 0xffffu; r[2] = bytes; r[3] = 0x00020000u; return r;
}
__device__ __forceinline__ void dma16(u32 voff, u32x4 rsrc, u32 soff, u32 dst) {
  u32 keep;
  asm volatile("s_mov_b32 %0, m0\n\ts_mov_b32 m0, %4\n\ts_nop 0\n\t"
               "buffer_load_dwordx4 %1, %2, %3 offen lds\n\ts_mov_b32 m0, %0"
               : "=&s"(keep) : "v"(voff), "s"(rsrc), "s"(soff), "s"(dst) : "memory");
}

__global__ __launch_bounds__(512, 2)
void lnlin_kernel(const u16* __restrict__ x, const u16* __restrict__ g, const u16* __restrict__ b,
                  const u16* __restrict__ w, u16* __restrict__ out, unsigned long long* stamps) {
  extern __shared__ __attribute__((aligned(16))) char smem[];
  const int bid = blockIdx.x, xcd = bid & 7, idx = bid >> 3;
  const int rp = xcd * 4 + (idx >> 3), cp = idx & 7;
  const int tid = threadIdx.x, lane = tid & 63;
  const int wid = __builtin_amdgcn_readfirstlane(tid >> 6);
  ST(0);
  const int rot = xcd * 2 + ((cp + (idx >> 3)) & 1) * 8;
  const u32 lds0 = (u32)(size_t)smem;

#pragma unroll 1
  for (int rep = 0; rep < REP; ++rep) {
  if (wid < 4) {
    const int wm = wid >> 1, wn = wid & 1;
    const int dr = lane >> 4;
    const u32 wvoff = (u32)(((wid * 16 + dr) * ND + cp * 128 + 8 * ((lane & 15) ^ (dr << 2))) * 2);
    const u32x4 wrsrc = mk_rsrc(w, KD * ND * 2);
    auto issueB = [&](int u) {
      const u32 s0 = u < 16 ? (u32)(((u + rot) & 15) * 64 * ND * 2) : OOB;
      const u32 dst = lds0 + OFF_B + (u & (SB - 1)) * 16384 + wid * 4096;
#pragma unroll
      for (int i = 0; i < 4; ++i) dma16(wvoff, wrsrc, s0 + i * 4 * ND * 2, dst + i * 1024);
    };
    {
      const u32x4 trsrc = mk_rsrc(wid < 2 ? g : b, KD * 2);
      dma16(lane * 16, trsrc, (wid & 1) * 1024, lds0 + (wid < 2 ? OFF_G16 : OFF_B16) + (wid & 1) * 1024);
      dma16(lane * 16, trsrc, OOB, lds0 + OFF_Z16 + (wid & 1) * 1024);
    }
#pragma unroll
    for (int u = 0; u < SB - 1; ++u) issueB(u);

    const int h = lane >> 5;
    const int xr = (lane >> 1) & 7;
    u32 xaddr[4];
#pragma unroll
    for (int s = 0; s < 4; ++s) xaddr[s] = OFF_A + (wm * 64 + (lane & 31)) * 128 + (((2 * s + h) ^ xr) * 16);
    const int blk = (lane >> 4) & 1, q = (lane & 15) >> 2, p4 = lane & 3;
    u32 waddr[2];
#pragma unroll
    for (int nt = 0; nt < 2; ++nt)
      waddr[nt] = OFF_B + (8 * h + q) * 256 + 16 * (4 * ((wn * 2 + (nt ^ wm)) ^ q) + 2 * blk + (p4 >> 1)) + 8 * (p4 & 1);
    const int jp = lane & 15, kg = lane >> 4;
    u32 seladdr = OFF_Z16;
    if ((jp & 1) == (kg & 1) && jp < 4) seladdr = (jp < 2 ? OFF_G16 : OFF_B16) + (kg >> 1) * 16;

    f32x16 acc[2][2] = {};
    f32x4 hacc = {0.f, 0.f, 0.f, 0.f};

    wait_vm<(SB - 2) * 4>();
    bar();
    ST(1);
#pragma unroll 1
    for (int u = 0; u < 16; ++u) {
      issueB(u + SB - 1);
      const int kt = (u + rot) & 15;
      const char* sa = smem + (u & 1) * 16384;
      const char* sb = smem + (u & (SB - 1)) * 16384;
      const char* sel = smem + seladdr + kt * 128;
      bf16x8 xf[2][2], wf[2][2], sf[2];
      auto ldfrag = [&](int s) {
#pragma unroll
        for (int nt = 0; nt < 2; ++nt) {
          s16x4 lo = __builtin_amdgcn_ds_read_tr16_b64_v4i16((lds_s16x4_p)(sb + waddr[nt] + s * 4096));
          s16x4 hi = __builtin_amdgcn_ds_read_tr16_b64_v4i16((lds_s16x4_p)(sb + waddr[nt] + s * 4096 + 1024));
          wf[s & 1][nt] = __builtin_shufflevector(lo, hi, 0, 1, 2, 3, 4, 5, 6, 7);
          xf[s & 1][nt] = *(const bf16x8*)(sa + xaddr[s] + nt * 4096);
        }
        sf[s & 1] = *(const bf16x8*)(sel + s * 32);
      };
      ldfrag(0);
#pragma unroll
      for (int s = 0; s < 4; ++s) {
        if (s < 3) ldfrag(s + 1);
        __builtin_amdgcn_sched_barrier(0);
#pragma unroll
        for (int mt = 0; mt < 2; ++mt)
#pragma unroll
          for (int nt = 0; nt < 2; ++nt)
            acc[mt][nt] = __builtin_amdgcn_mfma_f32_32x32x16_bf16(wf[s & 1][nt], xf[s & 1][mt], acc[mt][nt], 0, 0, 0);
        hacc = __builtin_amdgcn_mfma_f32_16x16x32_bf16(wf[s & 1][0], sf[s & 1], hacc, 0, 0, 0);
        __builtin_amdgcn_sched_barrier(0);
      }
      if (u == 15) break;
      wait_vm<(SB - 2) * 4>();
      wait_lgkm0();
      bar();
      ST(u + 2);
    }
    ST(17);
    if (jp < 4) *(f32x4*)(smem + OFF_C + (jp >> 1) * 512 + (wn * 64 + wm * 32 + (jp & 1) * 16 + 4 * kg) * 4) = hacc;
    wait_lgkm0();
    bar();
    ST(18);
#pragma unroll
    for (int mt = 0; mt < 2; ++mt) {
      const int lr = wm * 64 + mt * 32 + (lane & 31);
      const float2 st = *(const float2*)(smem + OFF_ST + lr * 8);
      u16* orow = out + (size_t)(rp * 128 + lr) * ND + cp * 128 + wn * 64 + h * 8;
#pragma unroll
      for (int nt = 0; nt < 2; ++nt) {
        u32 d[4][2];
#pragma unroll
        for (int qq = 0; qq < 4; ++qq) {
          const int n0 = wn * 64 + (nt ^ wm) * 32 + 8 * qq + 4 * h;
          const f32x4 c1 = *(const f32x4*)(smem + OFF_C + n0 * 4);
          const f32x4 c2 = *(const f32x4*)(smem + OFF_C + 512 + n0 * 4);
          float o[4];
#pragma unroll
          for (int e = 0; e < 4; ++e) o[e] = acc[mt][nt][4 * qq + e] * st.x + (c1[e] * st.y + c2[e]);
          d[qq][0] = cvt_pk(o[0], o[1]); d[qq][1] = cvt_pk(o[2], o[3]);
        }
#pragma unroll
        for (int qq = 0; qq < 4; qq += 2) {
          auto r0 = __builtin_amdgcn_permlane32_swap(d[qq][0], d[qq + 1][0], false, false);
          auto r1 = __builtin_amdgcn_permlane32_swap(d[qq][1], d[qq + 1][1], false, false);
          u32x4 v = {r0[0], r1[0], r0[1], r1[1]};
          *(u32x4*)(orow + (nt ^ wm) * 32 + 8 * qq) = v;
        }
      }
    }
  } else {
    const int sw = wid - 4;
    const int c = lane & 7, rr = lane >> 3;
    const __amdgpu_buffer_rsrc_t xrsrc = __builtin_amdgcn_make_buffer_rsrc((void*)x, 0, MD * KD * 2, 0x00020000);
    const u32 xvoff = (u32)(((rp * 128 + sw * 32 + rr) * KD + c * 8) * 2);
    const u32x4 gv0 = *(const u32x4*)(g + lane * 8), gv1 = *(const u32x4*)(g + 512 + lane * 8);
    u32x4 xv[2][4];
    auto issueX = [&](int u, int i) {
      const u32 s0 = u < 16 ? (u32)(((u + rot) & 15) * 128) : OOB;
      xv[u & 1][i] = __builtin_amdgcn_raw_buffer_load_b128(xrsrc, xvoff, s0 + i * 8 * KD * 2, 0);
    };
#pragma unroll
    for (int u = 0; u < 2; ++u)
#pragma unroll
      for (int i = 0; i < 4; ++i) issueX(u, i);
    {
      char* gt = smem + OFF_G32 + sw * 4096 + lane * 32;
      u32x4 t;
      t[0] = gv0[0] << 16; t[1] = gv0[0] & 0xffff0000u; t[2] = gv0[1] << 16; t[3] = gv0[1] & 0xffff0000u; *(u32x4*)(gt) = t;
      t[0] = gv0[2] << 16; t[1] = gv0[2] & 0xffff0000u; t[2] = gv0[3] << 16; t[3] = gv0[3] & 0xffff0000u; *(u32x4*)(gt + 16) = t;
      t[0] = gv1[0] << 16; t[1] = gv1[0] & 0xffff0000u; t[2] = gv1[1] << 16; t[3] = gv1[1] & 0xffff0000u; *(u32x4*)(gt + 2048) = t;
      t[0] = gv1[2] << 16; t[1] = gv1[2] & 0xffff0000u; t[2] = gv1[3] << 16; t[3] = gv1[3] & 0xffff0000u; *(u32x4*)(gt + 2064) = t;
    }
    const u32 wbase = OFF_A + sw * 4096 + rr * 128 + ((c ^ (rr >> 1)) * 16);
    const u32 gbase = OFF_G32 + sw * 4096 + c * 32;
    float s1[4] = {0.f, 0.f, 0.f, 0.f}, s2[4] = {0.f, 0.f, 0.f, 0.f};
    auto scale = [&](int uu, int par) {
      const int kt = (uu + rot) & 15;
      const f32x4 g0 = *(const f32x4*)(smem + gbase + kt * 256);
      const f32x4 g1 = *(const f32x4*)(smem + gbase + kt * 256 + 16);
#pragma unroll
      for (int i = 0; i < 4; ++i) {
        const u32x4 v = xv[par][i];
        u32x4 o;
        float t0, t1, t2, t3;
#define SC2(OA, OB, VA, VB, GA, GB, GC, GD) \
        "v_lshlrev_b32 %6, 16, " VA "\n\t" \
        "v_and_b32 %7, %22, " VA "\n\t" \
        "v_lshlrev_b32 %8, 16, " VB "\n\t" \
        "v_and_b32 %9, %22, " VB "\n\t" \
        "v_add_f32 %4, %4, %6\n\t" \
        "v_fmac_f32 %5, %6, %6\n\t" \
        "v_mul_f32 %6, " GA ", %6\n\t" \
        "v_add_f32 %4, %4, %7\n\t" \
        "v_fmac_f32 %5, %7, %7\n\t" \
        "v_mul_f32 %7, " GB ", %7\n\t" \
        "v_add_f32 %4, %4, %8\n\t" \
        "v_fmac_f32 %5, %8, %8\n\t" \
        "v_mul_f32 %8, " GC ", %8\n\t" \
        "v_add_f32 %4, %4, %9\n\t" \
        "v_fmac_f32 %5, %9, %9\n\t" \
        "v_mul_f32 %9, " GD ", %9\n\t" \
        "v_cvt_pk_bf16_f32 " OA ", %6, %7\n\t" \
        "v_cvt_pk_bf16_f32 " OB ", %8, %9\n\t"
        asm volatile(
          SC2("%0", "%1", "%10", "%11", "%14", "%15", "%16", "%17")
          SC2("%2", "%3", "%12", "%13", "%18", "%19", "%20", "%21")
          : "=&v"(o[0]), "=&v"(o[1]), "=&v"(o[2]), "=&v"(o[3]), "+v"(s1[i]), "+v"(s2[i]),
            "=&v"(t0), "=&v"(t1), "=&v"(t2), "=&v"(t3)
          : "v"(v[0]), "v"(v[1]), "v"(v[2]), "v"(v[3]),
            "v"(g0[0]), "v"(g0[1]), "v"(g0[2]), "v"(g0[3]), "v"(g1[0]), "v"(g1[1]), "v"(g1[2]), "v"(g1[3]),
            "s"(0xffff0000u));
        *(u32x4*)(smem + par * 16384 + ((wbase ^ ((i & 1) * 64)) + i * 1024)) = o;
        {
          const u32 s0 = (uu + 2) < 16 ? (u32)(((uu + 2 + rot) & 15) * 128) : OOB;
          xv[par][i] = __builtin_amdgcn_raw_buffer_load_b128(xrsrc, xvoff, s0 + i * 8 * KD * 2, 0);
        }
      }
    };
#pragma unroll 1
    for (int u = 0; u < 16; u += 2) {
      scale(u, 0);
      wait_lgkm0();
      ST(u + 1);
      bar();
      scale(u + 1, 1);
      wait_lgkm0();
      ST(u + 2);
      bar();
    }
    __builtin_amdgcn_s_waitcnt(0x0F70);
#pragma unroll
    for (int i = 0; i < 4; ++i) {
#pragma unroll
      for (int o = 1; o < 8; o <<= 1) { s1[i] += __shfl_xor(s1[i], o); s2[i] += __shfl_xor(s2[i], o); }
      const float mu = s1[i] * (1.f / KD);
      const float var = fmaxf(s2[i] * (1.f / KD) - mu * mu, 0.f);
      const float rs = rsqrtf(var + 1e-5f);
      if (c == 0) *(float2*)(smem + OFF_ST + (sw * 32 + i * 8 + rr) * 8) = make_float2(rs, -mu * rs);
    }
    wait_lgkm0();
    ST(18);
    bar();
    ST(17); ST(19);
    STDUMP();
  }
  __builtin_amdgcn_s_waitcnt(0x0070); bar();
  }
}

extern "C" void kernel_launch(void* const* d_in, const int* in_sizes, int n_in,
                              void* d_out, int out_size, void* d_ws, size_t ws_size, hipStream_t stream) {
  const u16* x = (const u16*)d_in[0]; const u16* g = (const u16*)d_in[1];
  const u16* b = (const u16*)d_in[2]; const u16* w = (const u16*)d_in[3];
  (void)hipFuncSetAttribute((const void*)lnlin_kernel, hipFuncAttributeMaxDynamicSharedMemorySize, LDS_BYTES);
  lnlin_kernel<<<256, 512, LDS_BYTES, stream>>>(x, g, b, w, (u16*)d_out, (unsigned long long*)((char*)d_ws + (64u << 20)));
}
```

```cpp
#define TOUCH 1
#include <hip/hip_runtime.h>
#include <hip/hip_bf16.h>
typedef unsigned int u32;
typedef unsigned short u16;
using bf16x8 = __attribute__((ext_vector_type(8))) short;
using s16x4  = __attribute__((ext_vector_type(4))) short;
using f32x4  = __attribute__((ext_vector_type(4))) float;
using f32x16 = __attribute__((ext_vector_type(16))) float;
using u32x4  = __attribute__((ext_vector_type(4))) u32;
using u32x2  = __attribute__((ext_vector_type(2))) u32;
typedef __attribute__((address_space(3))) s16x4* lds_s16x4_p;

#define ROTMODE 6

constexpr int MD = 4096, KD = 1024, ND = 1024;
constexpr int SA = 2, SB = 4;
constexpr int OFF_B = 0;
constexpr int OFF_A = OFF_B + SB * 16384;
constexpr int OFF_G32 = OFF_A + SA * 16384;
constexpr int OFF_G16 = OFF_G32 + 16384;
constexpr int OFF_B16 = OFF_G16 + 2048 + 64;
constexpr int OFF_Z16 = OFF_B16 + 2048 + 64;
constexpr int OFF_ST  = OFF_Z16 + 2048;
constexpr int OFF_C   = OFF_ST + 1024;
constexpr int OFF_STAMP = OFF_C + 1024;
constexpr int LDS_BYTES = OFF_STAMP + 512;
constexpr u32 OOB = 0x40000000u;

__device__ __forceinline__ void wait_lgkm0();
#define ST(i)
#define STDUMP()
template<int N> __device__ __forceinline__ void wait_vm() { asm volatile("s_waitcnt vmcnt(%0)" :: "i"(N) : "memory"); }
__device__ __forceinline__ void wait_lgkm0() { asm volatile("s_waitcnt lgkmcnt(0)" ::: "memory"); }
__device__ __forceinline__ void bar() { __builtin_amdgcn_s_barrier(); }
__device__ __forceinline__ u32 cvt_pk(float lo, float hi) {
  typedef __attribute__((ext_vector_type(2))) float f2; typedef __attribute__((ext_vector_type(2))) __bf16 b2;
  f2 v = {lo, hi}; b2 r = __builtin_convertvector(v, b2); return __builtin_bit_cast(u32, r);
}
__device__ __forceinline__ u32x4 mk_rsrc(const void* p, u32 bytes) {
  u32x4 r; r[0] = (u32)(size_t)p; r[1] = (u32)((size_t)p >> 32) & 0xffffu; r[2] = bytes; r[3] = 0x00020000u; return r;
}
__device__ __forceinline__ void dma16(u32 voff, u32x4 rsrc, u32 soff, u32 dst) {
  u32 keep;
  asm volatile("s_mov_b32 %0, m0\n\ts_mov_b32 m0, %4\n\ts_nop 0\n\t"
               "buffer_load_dwordx4 %1, %2, %3 offen lds\n\ts_mov_b32 m0, %0"
               : "=&s"(keep) : "v"(voff), "s"(rsrc), "s"(soff), "s"(dst) : "memory");
}

__global__ __launch_bounds__(576, 2)
void lnlin_kernel(const u16* __restrict__ x, const u16* __restrict__ g, const u16* __restrict__ b,
                  const u16* __restrict__ w, u16* __restrict__ out, unsigned long long* stamps) {
  extern __shared__ __attribute__((aligned(16))) char smem[];
  const int bid = blockIdx.x, xcd = bid & 7, idx = bid >> 3;
  const int rp = xcd * 4 + (idx >> 3), cp = idx & 7;
  const int tid = threadIdx.x, lane = tid & 63;
  const int wid = __builtin_amdgcn_readfirstlane(tid >> 6);
  ST(0);
  const int rot = xcd * 2 + ((cp + (idx >> 3)) & 1) * 8;
  const u32 lds0 = (u32)(size_t)smem;
  if (wid == 8) {
    const char* p;
    if (lane < 16) p = (const char*)w + (size_t)(idx * 16 + lane) * 4096;
    else if (lane < 24) p = (const char*)out + (size_t)xcd * 1048576 + (size_t)(idx * 8 + lane - 16) * 4096;
    else p = (const char*)x + (size_t)xcd * 1048576 + (size_t)(idx * 8 + ((lane - 24) & 7)) * 4096;
    u32 v = *(const volatile u32*)p;
    asm volatile("" :: "v"(v));
    return;
  }

  if (wid < 4) {
    const int wm = wid >> 1, wn = wid & 1;
    const int dr = lane >> 4;
    const u32 wvoff = (u32)(((wid * 16 + dr) * ND + cp * 128 + 8 * ((lane & 15) ^ (dr << 2))) * 2);
    const u32x4 wrsrc = mk_rsrc(w, KD * ND * 2);
    auto issueB = [&](int u) {
      const u32 s0 = u < 16 ? (u32)(((u + rot) & 15) * 64 * ND * 2) : OOB;
      const u32 dst = lds0 + OFF_B + (u & (SB - 1)) * 16384 + wid * 4096;
#pragma unroll
      for (int i = 0; i < 4; ++i) dma16(wvoff, wrsrc, s0 + i * 4 * ND * 2, dst + i * 1024);
    };
    {
      const u32x4 trsrc = mk_rsrc(wid < 2 ? g : b, KD * 2);
      dma16(lane * 16, trsrc, (wid & 1) * 1024, lds0 + (wid < 2 ? OFF_G16 : OFF_B16) + (wid & 1) * 1024);
      dma16(lane * 16, trsrc, OOB, lds0 + OFF_Z16 + (wid & 1) * 1024);
    }
#pragma unroll
    for (int u = 0; u < SB - 1; ++u) issueB(u);

    const int h = lane >> 5;
    const int xr = (lane >> 1) & 7;
    u32 xaddr[4];
#pragma unroll
    for (int s = 0; s < 4; ++s) xaddr[s] = OFF_A + (wm * 64 + (lane & 31)) * 128 + (((2 * s + h) ^ xr) * 16);
    const int blk = (lane >> 4) & 1, q = (lane & 15) >> 2, p4 = lane & 3;
    u32 waddr[2];
#pragma unroll
    for (int nt = 0; nt < 2; ++nt)
      waddr[nt] = OFF_B + (8 * h + q) * 256 + 16 * (4 * ((wn * 2 + (nt ^ wm)) ^ q) + 2 * blk + (p4 >> 1)) + 8 * (p4 & 1);
    const int jp = lane & 15, kg = lane >> 4;
    u32 seladdr = OFF_Z16;
    if ((jp & 1) == (kg & 1) && jp < 4) seladdr = (jp < 2 ? OFF_G16 : OFF_B16) + (kg >> 1) * 16;

    f32x16 acc[2][2] = {};
    f32x4 hacc = {0.f, 0.f, 0.f, 0.f};

    wait_vm<(SB - 2) * 4>();
    bar();
    ST(1);
#pragma unroll 1
    for (int u = 0; u < 16; ++u) {
      issueB(u + SB - 1);
      const int kt = (u + rot) & 15;
      const char* sa = smem + (u & 1) * 16384;
      const char* sb = smem + (u & (SB - 1)) * 16384;
      const char* sel = smem + seladdr + kt * 128;
      bf16x8 xf[2][2], wf[2][2], sf[2];
      auto ldfrag = [&](int s) {
#pragma unroll
        for (int nt = 0; nt < 2; ++nt) {
          s16x4 lo = __builtin_amdgcn_ds_read_tr16_b64_v4i16((lds_s16x4_p)(sb + waddr[nt] + s * 4096));
          s16x4 hi = __builtin_amdgcn_ds_read_tr16_b64_v4i16((lds_s16x4_p)(sb + waddr[nt] + s * 4096 + 1024));
          wf[s & 1][nt] = __builtin_shufflevector(lo, hi, 0, 1, 2, 3, 4, 5, 6, 7);
          xf[s & 1][nt] = *(const bf16x8*)(sa + xaddr[s] + nt * 4096);
        }
        sf[s & 1] = *(const bf16x8*)(sel + s * 32);
      };
      ldfrag(0);
#pragma unroll
      for (int s = 0; s < 4; ++s) {
        if (s < 3) ldfrag(s + 1);
        __builtin_amdgcn_sched_barrier(0);
#pragma unroll
        for (int mt = 0; mt < 2; ++mt)
#pragma unroll
          for (int nt = 0; nt < 2; ++nt)
            acc[mt][nt] = __builtin_amdgcn_mfma_f32_32x32x16_bf16(wf[s & 1][nt], xf[s & 1][mt], acc[mt][nt], 0, 0, 0);
        hacc = __builtin_amdgcn_mfma_f32_16x16x32_bf16(wf[s & 1][0], sf[s & 1], hacc, 0, 0, 0);
        __builtin_amdgcn_sched_barrier(0);
      }
      if (u == 15) break;
      wait_vm<(SB - 2) * 4>();
      wait_lgkm0();
      bar();
      ST(u + 2);
    }
    ST(17);
    if (jp < 4) *(f32x4*)(smem + OFF_C + (jp >> 1) * 512 + (wn * 64 + wm * 32 + (jp & 1) * 16 + 4 * kg) * 4) = hacc;
    wait_lgkm0();
    bar();
    ST(18);
#pragma unroll
    for (int mt = 0; mt < 2; ++mt) {
      const int lr = wm * 64 + mt * 32 + (lane & 31);
      const float2 st = *(const float2*)(smem + OFF_ST + lr * 8);
      u16* orow = out + (size_t)(rp * 128 + lr) * ND + cp * 128 + wn * 64 + h * 8;
#pragma unroll
      for (int nt = 0; nt < 2; ++nt) {
        u32 d[4][2];
#pragma unroll
        for (int qq = 0; qq < 4; ++qq) {
          const int n0 = wn * 64 + (nt ^ wm) * 32 + 8 * qq + 4 * h;
          const f32x4 c1 = *(const f32x4*)(smem + OFF_C + n0 * 4);
          const f32x4 c2 = *(const f32x4*)(smem + OFF_C + 512 + n0 * 4);
          float o[4];
#pragma unroll
          for (int e = 0; e < 4; ++e) o[e] = acc[mt][nt][4 * qq + e] * st.x + (c1[e] * st.y + c2[e]);
          d[qq][0] = cvt_pk(o[0], o[1]); d[qq][1] = cvt_pk(o[2], o[3]);
        }
#pragma unroll
        for (int qq = 0; qq < 4; qq += 2) {
          auto r0 = __builtin_amdgcn_permlane32_swap(d[qq][0], d[qq + 1][0], false, false);
          auto r1 = __builtin_amdgcn_permlane32_swap(d[qq][1], d[qq + 1][1], false, false);
          u32x4 v = {r0[0], r1[0], r0[1], r1[1]};
          *(u32x4*)(orow + (nt ^ wm) * 32 + 8 * qq) = v;
        }
      }
    }
  } else {
    const int sw = wid - 4;
    const int c = lane & 7, rr = lane >> 3;
    const __amdgpu_buffer_rsrc_t xrsrc = __builtin_amdgcn_make_buffer_rsrc((void*)x, 0, MD * KD * 2, 0x00020000);
    const u32 xvoff = (u32)(((rp * 128 + sw * 32 + rr) * KD + c * 8) * 2);
    const u32x4 gv0 = *(const u32x4*)(g + lane * 8), gv1 = *(const u32x4*)(g + 512 + lane * 8);
    u32x4 xv[2][4];
    auto issueX = [&](int u, int i) {
      const u32 s0 = u < 16 ? (u32)(((u + rot) & 15) * 128) : OOB;
      xv[u & 1][i] = __builtin_amdgcn_raw_buffer_load_b128(xrsrc, xvoff, s0 + i * 8 * KD * 2, 0);
    };
#pragma unroll
    for (int u = 0; u < 2; ++u)
#pragma unroll
      for (int i = 0; i < 4; ++i) issueX(u, i);
    {
      char* gt = smem + OFF_G32 + sw * 4096 + lane * 32;
      u32x4 t;
      t[0] = gv0[0] << 16; t[1] = gv0[0] & 0xffff0000u; t[2] = gv0[1] << 16; t[3] = gv0[1] & 0xffff0000u; *(u32x4*)(gt) = t;
      t[0] = gv0[2] << 16; t[1] = gv0[2] & 0xffff0000u; t[2] = gv0[3] << 16; t[3] = gv0[3] & 0xffff0000u; *(u32x4*)(gt + 16) = t;
      t[0] = gv1[0] << 16; t[1] = gv1[0] & 0xffff0000u; t[2] = gv1[1] << 16; t[3] = gv1[1] & 0xffff0000u; *(u32x4*)(gt + 2048) = t;
      t[0] = gv1[2] << 16; t[1] = gv1[2] & 0xffff0000u; t[2] = gv1[3] << 16; t[3] = gv1[3] & 0xffff0000u; *(u32x4*)(gt + 2064) = t;
    }
    const u32 wbase = OFF_A + sw * 4096 + rr * 128 + ((c ^ (rr >> 1)) * 16);
    const u32 gbase = OFF_G32 + sw * 4096 + c * 32;
    float s1[4] = {0.f, 0.f, 0.f, 0.f}, s2[4] = {0.f, 0.f, 0.f, 0.f};
    auto scale = [&](int uu, int par) {
      const int kt = (uu + rot) & 15;
      const f32x4 g0 = *(const f32x4*)(smem + gbase + kt * 256);
      const f32x4 g1 = *(const f32x4*)(smem + gbase + kt * 256 + 16);
#pragma unroll
      for (int i = 0; i < 4; ++i) {
        const u32x4 v = xv[par][i];
        u32x4 o;
        float t0, t1, t2, t3;
#define SC2(OA, OB, VA, VB, GA, GB, GC, GD) \
        "v_lshlrev_b32 %6, 16, " VA "\n\t" \
        "v_and_b32 %7, %22, " VA "\n\t" \
        "v_lshlrev_b32 %8, 16, " VB "\n\t" \
        "v_and_b32 %9, %22, " VB "\n\t" \
        "v_add_f32 %4, %4, %6\n\t" \
        "v_fmac_f32 %5, %6, %6\n\t" \
        "v_mul_f32 %6, " GA ", %6\n\t" \
        "v_add_f32 %4, %4, %7\n\t" \
        "v_fmac_f32 %5, %7, %7\n\t" \
        "v_mul_f32 %7, " GB ", %7\n\t" \
        "v_add_f32 %4, %4, %8\n\t" \
        "v_fmac_f32 %5, %8, %8\n\t" \
        "v_mul_f32 %8, " GC ", %8\n\t" \
        "v_add_f32 %4, %4, %9\n\t" \
        "v_fmac_f32 %5, %9, %9\n\t" \
        "v_mul_f32 %9, " GD ", %9\n\t" \
        "v_cvt_pk_bf16_f32 " OA ", %6, %7\n\t" \
        "v_cvt_pk_bf16_f32 " OB ", %8, %9\n\t"
        asm volatile(
          SC2("%0", "%1", "%10", "%11", "%14", "%15", "%16", "%17")
          SC2("%2", "%3", "%12", "%13", "%18", "%19", "%20", "%21")
          : "=&v"(o[0]), "=&v"(o[1]), "=&v"(o[2]), "=&v"(o[3]), "+v"(s1[i]), "+v"(s2[i]),
            "=&v"(t0), "=&v"(t1), "=&v"(t2), "=&v"(t3)
          : "v"(v[0]), "v"(v[1]), "v"(v[2]), "v"(v[3]),
            "v"(g0[0]), "v"(g0[1]), "v"(g0[2]), "v"(g0[3]), "v"(g1[0]), "v"(g1[1]), "v"(g1[2]), "v"(g1[3]),
            "s"(0xffff0000u));
        *(u32x4*)(smem + par * 16384 + ((wbase ^ ((i & 1) * 64)) + i * 1024)) = o;
        {
          const u32 s0 = (uu + 2) < 16 ? (u32)(((uu + 2 + rot) & 15) * 128) : OOB;
          xv[par][i] = __builtin_amdgcn_raw_buffer_load_b128(xrsrc, xvoff, s0 + i * 8 * KD * 2, 0);
        }
      }
    };
#pragma unroll 1
    for (int u = 0; u < 16; u += 2) {
      scale(u, 0);
      wait_lgkm0();
      ST(u + 1);
      bar();
      scale(u + 1, 1);
      wait_lgkm0();
      ST(u + 2);
      bar();
    }
    __builtin_amdgcn_s_waitcnt(0x0F70);
#pragma unroll
    for (int i = 0; i < 4; ++i) {
#pragma unroll
      for (int o = 1; o < 8; o <<= 1) { s1[i] += __shfl_xor(s1[i], o); s2[i] += __shfl_xor(s2[i], o); }
      const float mu = s1[i] * (1.f / KD);
      const float var = fmaxf(s2[i] * (1.f / KD) - mu * mu, 0.f);
      const float rs = rsqrtf(var + 1e-5f);
      if (c == 0) *(float2*)(smem + OFF_ST + (sw * 32 + i * 8 + rr) * 8) = make_float2(rs, -mu * rs);
    }
    wait_lgkm0();
    ST(18);
    bar();
    ST(17); ST(19);
    STDUMP();
  }
}

extern "C" void kernel_launch(void* const* d_in, const int* in_sizes, int n_in,
                              void* d_out, int out_size, void* d_ws, size_t ws_size, hipStream_t stream) {
  const u16* x = (const u16*)d_in[0]; const u16* g = (const u16*)d_in[1];
  const u16* b = (const u16*)d_in[2]; const u16* w = (const u16*)d_in[3];
  (void)hipFuncSetAttribute((const void*)lnlin_kernel, hipFuncAttributeMaxDynamicSharedMemorySize, LDS_BYTES);
  lnlin_kernel<<<256, 576, LDS_BYTES, stream>>>(x, g, b, w, (u16*)d_out, (unsigned long long*)((char*)d_ws + (64u << 20)));
}
```

```cpp
#include <hip/hip_runtime.h>
#include <hip/hip_bf16.h>
typedef unsigned int u32;
typedef unsigned short u16;
using bf16x8 = __attribute__((ext_vector_type(8))) short;
using s16x4  = __attribute__((ext_vector_type(4))) short;
using f32x4  = __attribute__((ext_vector_type(4))) float;
using f32x16 = __attribute__((ext_vector_type(16))) float;
using u32x4  = __attribute__((ext_vector_type(4))) u32;
using u32x2  = __attribute__((ext_vector_type(2))) u32;
typedef __attribute__((address_space(3))) s16x4* lds_s16x4_p;

#define ROTMODE 6

constexpr int MD = 4096, KD = 1024, ND = 1024;
constexpr int SA = 2, SB = 4;
constexpr int OFF_B = 0;
constexpr int OFF_A = OFF_B + SB * 16384;
constexpr int OFF_G32 = OFF_A + SA * 16384;
constexpr int OFF_G16 = OFF_G32 + 16384;
constexpr int OFF_B16 = OFF_G16 + 2048 + 64;
constexpr int OFF_Z16 = OFF_B16 + 2048 + 64;
constexpr int OFF_ST  = OFF_Z16 + 2048;
constexpr int OFF_C   = OFF_ST + 1024;
constexpr int OFF_STAMP = OFF_C + 1024;
constexpr int LDS_BYTES = OFF_STAMP + 512;
constexpr u32 OOB = 0x40000000u;

__device__ __forceinline__ void wait_lgkm0();
#define ST(i)
#define STDUMP()
template<int N> __device__ __forceinline__ void wait_vm() { asm volatile("s_waitcnt vmcnt(%0)" :: "i"(N) : "memory"); }
__device__ __forceinline__ void wait_lgkm0() { asm volatile("s_waitcnt lgkmcnt(0)" ::: "memory"); }
__device__ __forceinline__ void bar() { __builtin_amdgcn_s_barrier(); }
__device__ __forceinline__ u32 cvt_pk(float lo, float hi) {
  typedef __attribute__((ext_vector_type(2))) float f2; typedef __attribute__((ext_vector_type(2))) __bf16 b2;
  f2 v = {lo, hi}; b2 r = __builtin_convertvector(v, b2); return __builtin_bit_cast(u32, r);
}
__device__ __forceinline__ u32x4 mk_rsrc(const void* p, u32 bytes) {
  u32x4 r; r[0] = (u32)(size_t)p; r[1] = (u32)((size_t)p >> 32) & 0xffffu; r[2] = bytes; r[3] = 0x00020000u; return r;
}
__device__ __forceinline__ void dma16(u32 voff, u32x4 rsrc, u32 soff, u32 dst) {
  u32 keep;
  asm volatile("s_mov_b32 %0, m0\n\ts_mov_b32 m0, %4\n\ts_nop 0\n\t"
               "buffer_load_dwordx4 %1, %2, %3 offen lds\n\ts_mov_b32 m0, %0"
               : "=&s"(keep) : "v"(voff), "s"(rsrc), "s"(soff), "s"(dst) : "memory");
}

__global__ __launch_bounds__(512, 2)
void lnlin_kernel(const u16* __restrict__ x, const u16* __restrict__ g, const u16* __restrict__ b,
                  const u16* __restrict__ w, u16* __restrict__ out, unsigned long long* stamps) {
  extern __shared__ __attribute__((aligned(16))) char smem[];
  const int bid = blockIdx.x, xcd = bid & 7, idx = bid >> 3;
  const int rp = xcd * 4 + (idx >> 3), cp = idx & 7;
  const int tid = threadIdx.x, lane = tid & 63;
  const int wid = __builtin_amdgcn_readfirstlane(tid >> 6);
  ST(0);
  const int rot = xcd * 2 + ((cp + (idx >> 3)) & 1) * 8;
  const u32 lds0 = (u32)(size_t)smem;
  const unsigned long long tstart_ = __builtin_amdgcn_s_memrealtime();
#define PADTO(T) do{ while (__builtin_amdgcn_s_memrealtime() - tstart_ < (T)) __builtin_amdgcn_s_sleep(1); }while(0)

  if (wid < 4) {
    const int wm = wid >> 1, wn = wid & 1;
    const int dr = lane >> 4;
    const u32 wvoff = (u32)(((wid * 16 + dr) * ND + cp * 128 + 8 * ((lane & 15) ^ (dr << 2))) * 2);
    const u32x4 wrsrc = mk_rsrc(w, KD * ND * 2);
    auto issueB = [&](int u) {
      const u32 s0 = u < 16 ? (u32)(((u + rot) & 15) * 64 * ND * 2) : OOB;
      const u32 dst = lds0 + OFF_B + (u & (SB - 1)) * 16384 + wid * 4096;
#pragma unroll
      for (int i = 0; i < 4; ++i) dma16(wvoff, wrsrc, s0 + i * 4 * ND * 2, dst + i * 1024);
    };
    {
      const u32x4 trsrc = mk_rsrc(wid < 2 ? g : b, KD * 2);
      dma16(lane * 16, trsrc, (wid & 1) * 1024, lds0 + (wid < 2 ? OFF_G16 : OFF_B16) + (wid & 1) * 1024);
      dma16(lane * 16, trsrc, OOB, lds0 + OFF_Z16 + (wid & 1) * 1024);
    }
#pragma unroll
    for (int u = 0; u < SB - 1; ++u) issueB(u);

    const int h = lane >> 5;
    const int xr = (lane >> 1) & 7;
    u32 xaddr[4];
#pragma unroll
    for (int s = 0; s < 4; ++s) xaddr[s] = OFF_A + (wm * 64 + (lane & 31)) * 128 + (((2 * s + h) ^ xr) * 16);
    const int blk = (lane >> 4) & 1, q = (lane & 15) >> 2, p4 = lane & 3;
    u32 waddr[2];
#pragma unroll
    for (int nt = 0; nt < 2; ++nt)
      waddr[nt] = OFF_B + (8 * h + q) * 256 + 16 * (4 * ((wn * 2 + (nt ^ wm)) ^ q) + 2 * blk + (p4 >> 1)) + 8 * (p4 & 1);
    const int jp = lane & 15, kg = lane >> 4;
    u32 seladdr = OFF_Z16;
    if ((jp & 1) == (kg & 1) && jp < 4) seladdr = (jp < 2 ? OFF_G16 : OFF_B16) + (kg >> 1) * 16;

    f32x16 acc[2][2] = {};
    f32x4 hacc = {0.f, 0.f, 0.f, 0.f};

    wait_vm<(SB - 2) * 4>();
    bar();
    ST(1);
#pragma unroll 1
    for (int u = 0; u < 16; ++u) {
      issueB(u + SB - 1);
      const int kt = (u + rot) & 15;
      const char* sa = smem + (u & 1) * 16384;
      const char* sb = smem + (u & (SB - 1)) * 16384;
      const char* sel = smem + seladdr + kt * 128;
      bf16x8 xf[2][2], wf[2][2], sf[2];
      auto ldfrag = [&](int s) {
#pragma unroll
        for (int nt = 0; nt < 2; ++nt) {
          s16x4 lo = __builtin_amdgcn_ds_read_tr16_b64_v4i16((lds_s16x4_p)(sb + waddr[nt] + s * 4096));
          s16x4 hi = __builtin_amdgcn_ds_read_tr16_b64_v4i16((lds_s16x4_p)(sb + waddr[nt] + s * 4096 + 1024));
          wf[s & 1][nt] = __builtin_shufflevector(lo, hi, 0, 1, 2, 3, 4, 5, 6, 7);
          xf[s & 1][nt] = *(const bf16x8*)(sa + xaddr[s] + nt * 4096);
        }
        sf[s & 1] = *(const bf16x8*)(sel + s * 32);
      };
      ldfrag(0);
#pragma unroll
      for (int s = 0; s < 4; ++s) {
        if (s < 3) ldfrag(s + 1);
        __builtin_amdgcn_sched_barrier(0);
#pragma unroll
        for (int mt = 0; mt < 2; ++mt)
#pragma unroll
          for (int nt = 0; nt < 2; ++nt)
            acc[mt][nt] = __builtin_amdgcn_mfma_f32_32x32x16_bf16(wf[s & 1][nt], xf[s & 1][mt], acc[mt][nt], 0, 0, 0);
        hacc = __builtin_amdgcn_mfma_f32_16x16x32_bf16(wf[s & 1][0], sf[s & 1], hacc, 0, 0, 0);
        __builtin_amdgcn_sched_barrier(0);
      }
      if (u == 15) break;
      wait_vm<(SB - 2) * 4>();
      wait_lgkm0();
      bar();
      ST(u + 2);
    }
    ST(17);
    if (jp < 4) *(f32x4*)(smem + OFF_C + (jp >> 1) * 512 + (wn * 64 + wm * 32 + (jp & 1) * 16 + 4 * kg) * 4) = hacc;
    wait_lgkm0();
    bar();
    ST(18);
#pragma unroll
    for (int mt = 0; mt < 2; ++mt) {
      const int lr = wm * 64 + mt * 32 + (lane & 31);
      const float2 st = *(const float2*)(smem + OFF_ST + lr * 8);
      u16* orow = out + (size_t)(rp * 128 + lr) * ND + cp * 128 + wn * 64 + h * 8;
#pragma unroll
      for (int nt = 0; nt < 2; ++nt) {
        u32 d[4][2];
#pragma unroll
        for (int qq = 0; qq < 4; ++qq) {
          const int n0 = wn * 64 + (nt ^ wm) * 32 + 8 * qq + 4 * h;
          const f32x4 c1 = *(const f32x4*)(smem + OFF_C + n0 * 4);
          const f32x4 c2 = *(const f32x4*)(smem + OFF_C + 512 + n0 * 4);
          float o[4];
#pragma unroll
          for (int e = 0; e < 4; ++e) o[e] = acc[mt][nt][4 * qq + e] * st.x + (c1[e] * st.y + c2[e]);
          d[qq][0] = cvt_pk(o[0], o[1]); d[qq][1] = cvt_pk(o[2], o[3]);
        }
#pragma unroll
        for (int qq = 0; qq < 4; qq += 2) {
          auto r0 = __builtin_amdgcn_permlane32_swap(d[qq][0], d[qq + 1][0], false, false);
          auto r1 = __builtin_amdgcn_permlane32_swap(d[qq][1], d[qq + 1][1], false, false);
          u32x4 v = {r0[0], r1[0], r0[1], r1[1]};
          { u16* sp_ = orow + (nt ^ wm) * 32 + 8 * qq; asm volatile("global_store_dwordx4 %0, %1, off sc0 sc1\n\ts_nop 1" :: "v"(sp_), "v"(v) : "memory"); }
        }
      }
    }
    wait_vm<0>(); PADTO(3000);
  } else {
    const int sw = wid - 4;
    const int c = lane & 7, rr = lane >> 3;
    const __amdgpu_buffer_rsrc_t xrsrc = __builtin_amdgcn_make_buffer_rsrc((void*)x, 0, MD * KD * 2, 0x00020000);
    const u32 xvoff = (u32)(((rp * 128 + sw * 32 + rr) * KD + c * 8) * 2);
    const u32x4 gv0 = *(const u32x4*)(g + lane * 8), gv1 = *(const u32x4*)(g + 512 + lane * 8);
    u32x4 xv[2][4];
    auto issueX = [&](int u, int i) {
      const u32 s0 = u < 16 ? (u32)(((u + rot) & 15) * 128) : OOB;
      xv[u & 1][i] = __builtin_amdgcn_raw_buffer_load_b128(xrsrc, xvoff, s0 + i * 8 * KD * 2, 0);
    };
#pragma unroll
    for (int u = 0; u < 2; ++u)
#pragma unroll
      for (int i = 0; i < 4; ++i) issueX(u, i);
    {
      char* gt = smem + OFF_G32 + sw * 4096 + lane * 32;
      u32x4 t;
      t[0] = gv0[0] << 16; t[1] = gv0[0] & 0xffff0000u; t[2] = gv0[1] << 16; t[3] = gv0[1] & 0xffff0000u; *(u32x4*)(gt) = t;
      t[0] = gv0[2] << 16; t[1] = gv0[2] & 0xffff0000u; t[2] = gv0[3] << 16; t[3] = gv0[3] & 0xffff0000u; *(u32x4*)(gt + 16) = t;
      t[0] = gv1[0] << 16; t[1] = gv1[0] & 0xffff0000u; t[2] = gv1[1] << 16; t[3] = gv1[1] & 0xffff0000u; *(u32x4*)(gt + 2048) = t;
      t[0] = gv1[2] << 16; t[1] = gv1[2] & 0xffff0000u; t[2] = gv1[3] << 16; t[3] = gv1[3] & 0xffff0000u; *(u32x4*)(gt + 2064) = t;
    }
    const u32 wbase = OFF_A + sw * 4096 + rr * 128 + ((c ^ (rr >> 1)) * 16);
    const u32 gbase = OFF_G32 + sw * 4096 + c * 32;
    float s1[4] = {0.f, 0.f, 0.f, 0.f}, s2[4] = {0.f, 0.f, 0.f, 0.f};
    auto scale = [&](int uu, int par) {
      const int kt = (uu + rot) & 15;
      const f32x4 g0 = *(const f32x4*)(smem + gbase + kt * 256);
      const f32x4 g1 = *(const f32x4*)(smem + gbase + kt * 256 + 16);
#pragma unroll
      for (int i = 0; i < 4; ++i) {
        const u32x4 v = xv[par][i];
        u32x4 o;
        float t0, t1, t2, t3;
#define SC2(OA, OB, VA, VB, GA, GB, GC, GD) \
        "v_lshlrev_b32 %6, 16, " VA "\n\t" \
        "v_and_b32 %7, %22, " VA "\n\t" \
        "v_lshlrev_b32 %8, 16, " VB "\n\t" \
        "v_and_b32 %9, %22, " VB "\n\t" \
        "v_add_f32 %4, %4, %6\n\t" \
        "v_fmac_f32 %5, %6, %6\n\t" \
        "v_mul_f32 %6, " GA ", %6\n\t" \
        "v_add_f32 %4, %4, %7\n\t" \
        "v_fmac_f32 %5, %7, %7\n\t" \
        "v_mul_f32 %7, " GB ", %7\n\t" \
        "v_add_f32 %4, %4, %8\n\t" \
        "v_fmac_f32 %5, %8, %8\n\t" \
        "v_mul_f32 %8, " GC ", %8\n\t" \
        "v_add_f32 %4, %4, %9\n\t" \
        "v_fmac_f32 %5, %9, %9\n\t" \
        "v_mul_f32 %9, " GD ", %9\n\t" \
        "v_cvt_pk_bf16_f32 " OA ", %6, %7\n\t" \
        "v_cvt_pk_bf16_f32 " OB ", %8, %9\n\t"
        asm volatile(
          SC2("%0", "%1", "%10", "%11", "%14", "%15", "%16", "%17")
          SC2("%2", "%3", "%12", "%13", "%18", "%19", "%20", "%21")
          : "=&v"(o[0]), "=&v"(o[1]), "=&v"(o[2]), "=&v"(o[3]), "+v"(s1[i]), "+v"(s2[i]),
            "=&v"(t0), "=&v"(t1), "=&v"(t2), "=&v"(t3)
          : "v"(v[0]), "v"(v[1]), "v"(v[2]), "v"(v[3]),
            "v"(g0[0]), "v"(g0[1]), "v"(g0[2]), "v"(g0[3]), "v"(g1[0]), "v"(g1[1]), "v"(g1[2]), "v"(g1[3]),
            "s"(0xffff0000u));
        *(u32x4*)(smem + par * 16384 + ((wbase ^ ((i & 1) * 64)) + i * 1024)) = o;
        {
          const u32 s0 = (uu + 2) < 16 ? (u32)(((uu + 2 + rot) & 15) * 128) : OOB;
          xv[par][i] = __builtin_amdgcn_raw_buffer_load_b128(xrsrc, xvoff, s0 + i * 8 * KD * 2, 0);
        }
      }
    };
#pragma unroll 1
    for (int u = 0; u < 16; u += 2) {
      scale(u, 0);
      wait_lgkm0();
      ST(u + 1);
      bar();
      scale(u + 1, 1);
      wait_lgkm0();
      ST(u + 2);
      bar();
    }
    __builtin_amdgcn_s_waitcnt(0x0F70);
#pragma unroll
    for (int i = 0; i < 4; ++i) {
#pragma unroll
      for (int o = 1; o < 8; o <<= 1) { s1[i] += __shfl_xor(s1[i], o); s2[i] += __shfl_xor(s2[i], o); }
      const float mu = s1[i] * (1.f / KD);
      const float var = fmaxf(s2[i] * (1.f / KD) - mu * mu, 0.f);
      const float rs = rsqrtf(var + 1e-5f);
      if (c == 0) *(float2*)(smem + OFF_ST + (sw * 32 + i * 8 + rr) * 8) = make_float2(rs, -mu * rs);
    }
    wait_lgkm0();
    ST(18);
    bar();
    ST(17); ST(19);
    STDUMP();
  }
}

extern "C" void kernel_launch(void* const* d_in, const int* in_sizes, int n_in,
                              void* d_out, int out_size, void* d_ws, size_t ws_size, hipStream_t stream) {
  const u16* x = (const u16*)d_in[0]; const u16* g = (const u16*)d_in[1];
  const u16* b = (const u16*)d_in[2]; const u16* w = (const u16*)d_in[3];
  (void)hipFuncSetAttribute((const void*)lnlin_kernel, hipFuncAttributeMaxDynamicSharedMemorySize, LDS_BYTES);
  lnlin_kernel<<<256, 512, LDS_BYTES, stream>>>(x, g, b, w, (u16*)d_out, (unsigned long long*)((char*)d_ws + (64u << 20)));
}
```

```cpp
#include <hip/hip_runtime.h>
#include <hip/hip_bf16.h>
typedef unsigned int u32;
typedef unsigned short u16;
using bf16x8 = __attribute__((ext_vector_type(8))) short;
using s16x4  = __attribute__((ext_vector_type(4))) short;
using f32x4  = __attribute__((ext_vector_type(4))) float;
using f32x16 = __attribute__((ext_vector_type(16))) float;
using u32x4  = __attribute__((ext_vector_type(4))) u32;
using u32x2  = __attribute__((ext_vector_type(2))) u32;
typedef __attribute__((address_space(3))) s16x4* lds_s16x4_p;

#define ROTMODE 6
#define STAUX 16

constexpr int MD = 4096, KD = 1024, ND = 1024;
constexpr int NS = 4;
constexpr int SLOT = 32768;
constexpr int OFF_G32 = NS * SLOT;
constexpr int OFF_G16 = OFF_G32 + 4096;
constexpr int OFF_B16 = OFF_G16 + 2048 + 64;
constexpr int OFF_Z16 = OFF_B16 + 2048 + 64;
constexpr int OFF_ST  = OFF_Z16 + 2048;
constexpr int OFF_C   = OFF_ST + 1024;
constexpr int OFF_SX  = OFF_C + 1024;
constexpr int OFF_STAMP = OFF_SX + 512;
constexpr int LDS_BYTES = OFF_STAMP + 512;
constexpr int OFF_OUT = SLOT;
constexpr u32 OOB = 0x40000000u;

template<int N> __device__ __forceinline__ void wait_vm() { asm volatile("s_waitcnt vmcnt(%0)" :: "i"(N) : "memory"); }
__device__ __forceinline__ void wait_lgkm0() { asm volatile("s_waitcnt lgkmcnt(0)" ::: "memory"); }
__device__ __forceinline__ void bar() { asm volatile("s_barrier" ::: "memory"); }
#define ST(i)
#define CT(i)
#define STDUMP()
__device__ __forceinline__ u32 cvt_pk(float lo, float hi) {
  typedef __attribute__((ext_vector_type(2))) float f2; typedef __attribute__((ext_vector_type(2))) __bf16 b2;
  f2 v = {lo, hi}; b2 r = __builtin_convertvector(v, b2); return __builtin_bit_cast(u32, r);
}
__device__ __forceinline__ u32x4 mk_rsrc(const void* p, u32 bytes) {
  u32x4 r; r[0] = (u32)(size_t)p; r[1] = (u32)((size_t)p >> 32) & 0xffffu; r[2] = bytes; r[3] = 0x00020000u; return r;
}
__device__ __forceinline__ void dma16(u32 voff, u32x4 rsrc, u32 soff, u32 dst) {
  u32 keep;
  asm volatile("s_mov_b32 %0, m0\n\ts_mov_b32 m0, %4\n\ts_nop 0\n\t"
               "buffer_load_dwordx4 %1, %2, %3 offen lds\n\ts_mov_b32 m0, %0"
               : "=&s"(keep) : "v"(voff), "s"(rsrc), "s"(soff), "s"(dst) : "memory");
}
__device__ __forceinline__ s16x4 lo4(bf16x8 v) { return __builtin_shufflevector(v, v, 0, 1, 2, 3); }
__device__ __forceinline__ s16x4 hi4(bf16x8 v) { return __builtin_shufflevector(v, v, 4, 5, 6, 7); }

__global__ __launch_bounds__(512, 2)
void lnlin_kernel(const u16* __restrict__ x, const u16* __restrict__ g, const u16* __restrict__ b,
                  const u16* __restrict__ w, u16* __restrict__ out, unsigned long long* stamps) {
  extern __shared__ __attribute__((aligned(16))) char smem[];
  const int bid = blockIdx.x, xcd = bid & 7, idx = bid >> 3;
  const int rp = xcd * 4 + (idx >> 3), cp = idx & 7;
  const int tid = threadIdx.x, lane = tid & 63;
  const int wid = __builtin_amdgcn_readfirstlane(tid >> 6);
  ST(0);
  const int rot = xcd * 2 + ((cp + (idx >> 3)) & 1) * 8;
  const u32 lds0 = (u32)(size_t)smem;
  const int h = lane >> 5;
  const int xr = (lane >> 1) & 7;
  const int blk = (lane >> 4) & 1, q = (lane & 15) >> 2, p4 = lane & 3;
  const int wq = wid & 3;
  const u32 waddr = 16384 + (8 * h + q) * 256 + 16 * (4 * (wq ^ q) + 2 * blk + (p4 >> 1)) + 8 * (p4 & 1);

  if (wid < 4) {
    {
      const u32x2 gv = *(const u32x2*)(g + tid * 4);
      u32x4 t; t[0] = gv[0] << 16; t[1] = gv[0] & 0xffff0000u; t[2] = gv[1] << 16; t[3] = gv[1] & 0xffff0000u;
      *(u32x4*)(smem + OFF_G32 + tid * 16) = t;
    }
    u32 xa0 = lds0 + (lane & 31) * 128 + (((0 + h) ^ xr) * 16), xa1 = lds0 + (lane & 31) * 128 + (((2 + h) ^ xr) * 16);
    u32 xa2 = lds0 + (lane & 31) * 128 + (((4 + h) ^ xr) * 16), xa3 = lds0 + (lane & 31) * 128 + (((6 + h) ^ xr) * 16);
    u32 wa = lds0 + waddr;
    u32 ga = lds0 + OFF_G32 + (rot & 15) * 256 + (16 * ((lane & 15) >> 3) + 8 * h + (lane & 7)) * 4;
    f32x16 acc0, acc1, acc2, acc3;
    ST(1); CT(3);
    asm volatile(
      "s_mov_b32 s21, 0xffff0000\n\t"
      "s_mov_b32 s22, 0x10000\n\t"
      "s_mov_b32 s23, 0xfff\n\t"
      "s_mov_b32 s20, 8\n\t"
      "v_mov_b32 v7, v6\n\t"
      "v_mov_b32 v66, 0\n\t"
      "v_mov_b32 v67, 0\n\t"
      "v_mov_b32 v68, 0\n\t"
      "v_mov_b32 v69, 0\n\t"
      "v_mov_b32 v70, 0\n\t"
      "v_mov_b32 v71, 0\n\t"
      "v_mov_b32 v72, 0\n\t"
      "v_mov_b32 v73, 0\n\t"
      "v_mov_b32 v74, 0\n\t"
      "v_mov_b32 v75, 0\n\t"
      "v_mov_b32 v76, 0\n\t"
      "v_mov_b32 v77, 0\n\t"
      "v_mov_b32 v78, 0\n\t"
      "v_mov_b32 v79, 0\n\t"
      "v_mov_b32 v80, 0\n\t"
      "v_mov_b32 v81, 0\n\t"
      "v_mov_b32 v82, 0\n\t"
      "v_mov_b32 v83, 0\n\t"
      "v_mov_b32 v84, 0\n\t"
      "v_mov_b32 v85, 0\n\t"
      "v_mov_b32 v86, 0\n\t"
      "v_mov_b32 v87, 0\n\t"
      "v_mov_b32 v88, 0\n\t"
      "v_mov_b32 v89, 0\n\t"
      "v_mov_b32 v90, 0\n\t"
      "v_mov_b32 v91, 0\n\t"
      "v_mov_b32 v92, 0\n\t"
      "v_mov_b32 v93, 0\n\t"
      "v_mov_b32 v94, 0\n\t"
      "v_mov_b32 v95, 0\n\t"
      "v_mov_b32 v96, 0\n\t"
      "v_mov_b32 v97, 0\n\t"
      "v_mov_b32 v98, 0\n\t"
      "v_mov_b32 v99, 0\n\t"
      "v_mov_b32 v100, 0\n\t"
      "v_mov_b32 v101, 0\n\t"
      "v_mov_b32 v102, 0\n\t"
      "v_mov_b32 v103, 0\n\t"
      "v_mov_b32 v104, 0\n\t"
      "v_mov_b32 v105, 0\n\t"
      "v_mov_b32 v106, 0\n\t"
      "v_mov_b32 v107, 0\n\t"
      "v_mov_b32 v108, 0\n\t"
      "v_mov_b32 v109, 0\n\t"
      "v_mov_b32 v110, 0\n\t"
      "v_mov_b32 v111, 0\n\t"
      "v_mov_b32 v112, 0\n\t"
      "v_mov_b32 v113, 0\n\t"
      "v_mov_b32 v114, 0\n\t"
      "v_mov_b32 v115, 0\n\t"
      "v_mov_b32 v116, 0\n\t"
      "v_mov_b32 v117, 0\n\t"
      "v_mov_b32 v118, 0\n\t"
      "v_mov_b32 v119, 0\n\t"
      "v_mov_b32 v120, 0\n\t"
      "v_mov_b32 v121, 0\n\t"
      "v_mov_b32 v122, 0\n\t"
      "v_mov_b32 v123, 0\n\t"
      "v_mov_b32 v124, 0\n\t"
      "v_mov_b32 v125, 0\n\t"
      "v_mov_b32 v126, 0\n\t"
      "v_mov_b32 v127, 0\n\t"
      "v_mov_b32 v128, 0\n\t"
      "v_mov_b32 v129, 0\n\t"
      "s_waitcnt lgkmcnt(0)\n\t"
      "s_barrier\n\t"
      "ds_read_b64_tr_b16 v[48:49], v5 offset:0\n\t"
      "ds_read_b64_tr_b16 v[50:51], v5 offset:1024\n\t"
      "ds_read_b32 v56, v6\n\t"
      "ds_read_b128 v[8:11], v1 offset:0\n\t"
      "ds_read_b128 v[12:15], v1 offset:4096\n\t"
      "ds_read_b128 v[16:19], v1 offset:8192\n\t"
      "ds_read_b128 v[20:23], v1 offset:12288\n\t"
      "ds_read_b64_tr_b16 v[52:53], v5 offset:4096\n\t"
      "ds_read_b64_tr_b16 v[54:55], v5 offset:5120\n\t"
      "ds_read_b32 v57, v6 offset:128\n\t"
      "s_waitcnt lgkmcnt(7)\n\t"
      "v_lshlrev_b32 v58, 16, v48\n\t"
      "v_and_b32 v59, s21, v48\n\t"
      "v_mul_f32_dpp v58, v56, v58 row_newbcast:0 row_mask:0xf bank_mask:0xf\n\t"
      "v_mul_f32_dpp v59, v56, v59 row_newbcast:1 row_mask:0xf bank_mask:0xf\n\t"
      "v_cvt_pk_bf16_f32 v40, v58, v59\n\t"
      "v_lshlrev_b32 v60, 16, v49\n\t"
      "v_and_b32 v61, s21, v49\n\t"
      "v_mul_f32_dpp v60, v56, v60 row_newbcast:2 row_mask:0xf bank_mask:0xf\n\t"
      "v_mul_f32_dpp v61, v56, v61 row_newbcast:3 row_mask:0xf bank_mask:0xf\n\t"
      "v_cvt_pk_bf16_f32 v41, v60, v61\n\t"
      "v_lshlrev_b32 v62, 16, v50\n\t"
      "v_and_b32 v63, s21, v50\n\t"
      "v_mul_f32_dpp v62, v56, v62 row_newbcast:4 row_mask:0xf bank_mask:0xf\n\t"
      "v_mul_f32_dpp v63, v56, v63 row_newbcast:5 row_mask:0xf bank_mask:0xf\n\t"
      "v_cvt_pk_bf16_f32 v42, v62, v63\n\t"
      "v_lshlrev_b32 v64, 16, v51\n\t"
      "v_and_b32 v65, s21, v51\n\t"
      "v_mul_f32_dpp v64, v56, v64 row_newbcast:6 row_mask:0xf bank_mask:0xf\n\t"
      "v_mul_f32_dpp v65, v56, v65 row_newbcast:7 row_mask:0xf bank_mask:0xf\n\t"
      "v_cvt_pk_bf16_f32 v43, v64, v65\n\t"
      "s_nop 0\n\t"
      "1:\n\t"
      "s_waitcnt lgkmcnt(0)\n\t"
      "v_mfma_f32_32x32x16_bf16 v[66:81], v[40:43], v[8:11], v[66:81]\n\t"
      "ds_read_b64_tr_b16 v[48:49], v5 offset:8192\n\t"
      "ds_read_b64_tr_b16 v[50:51], v5 offset:9216\n\t"
      "ds_read_b128 v[24:27], v2 offset:0\n\t"
      "v_lshlrev_b32 v58, 16, v52\n\t"
      "v_and_b32 v59, s21, v52\n\t"
      "v_mul_f32_dpp v58, v56, v58 row_newbcast:8 row_mask:0xf bank_mask:0xf\n\t"
      "v_mul_f32_dpp v59, v56, v59 row_newbcast:9 row_mask:0xf bank_mask:0xf\n\t"
      "v_mfma_f32_32x32x16_bf16 v[82:97], v[40:43], v[12:15], v[82:97]\n\t"
      "ds_read_b128 v[28:31], v2 offset:4096\n\t"
      "ds_read_b128 v[32:35], v2 offset:8192\n\t"
      "ds_read_b128 v[36:39], v2 offset:12288\n\t"
      "v_cvt_pk_bf16_f32 v44, v58, v59\n\t"
      "v_lshlrev_b32 v60, 16, v53\n\t"
      "v_and_b32 v61, s21, v53\n\t"
      "v_mul_f32_dpp v60, v56, v60 row_newbcast:10 row_mask:0xf bank_mask:0xf\n\t"
      "v_mfma_f32_32x32x16_bf16 v[98:113], v[40:43], v[16:19], v[98:113]\n\t"
      "v_mul_f32_dpp v61, v56, v61 row_newbcast:11 row_mask:0xf bank_mask:0xf\n\t"
      "v_cvt_pk_bf16_f32 v45, v60, v61\n\t"
      "v_lshlrev_b32 v62, 16, v54\n\t"
      "v_and_b32 v63, s21, v54\n\t"
      "v_mul_f32_dpp v62, v56, v62 row_newbcast:12 row_mask:0xf bank_mask:0xf\n\t"
      "v_mul_f32_dpp v63, v56, v63 row_newbcast:13 row_mask:0xf bank_mask:0xf\n\t"
      "v_mfma_f32_32x32x16_bf16 v[114:129], v[40:43], v[20:23], v[114:129]\n\t"
      "v_cvt_pk_bf16_f32 v46, v62, v63\n\t"
      "v_lshlrev_b32 v64, 16, v55\n\t"
      "v_and_b32 v65, s21, v55\n\t"
      "v_mul_f32_dpp v64, v56, v64 row_newbcast:14 row_mask:0xf bank_mask:0xf\n\t"
      "v_mul_f32_dpp v65, v56, v65 row_newbcast:15 row_mask:0xf bank_mask:0xf\n\t"
      "v_cvt_pk_bf16_f32 v47, v64, v65\n\t"
      "s_nop 0\n\t"
      "s_waitcnt lgkmcnt(0)\n\t"
      "v_mfma_f32_32x32x16_bf16 v[66:81], v[44:47], v[24:27], v[66:81]\n\t"
      "ds_read_b64_tr_b16 v[52:53], v5 offset:12288\n\t"
      "ds_read_b64_tr_b16 v[54:55], v5 offset:13312\n\t"
      "ds_read_b128 v[8:11], v3 offset:0\n\t"
      "v_lshlrev_b32 v58, 16, v48\n\t"
      "v_and_b32 v59, s21, v48\n\t"
      "v_mul_f32_dpp v58, v57, v58 row_newbcast:0 row_mask:0xf bank_mask:0xf\n\t"
      "v_mul_f32_dpp v59, v57, v59 row_newbcast:1 row_mask:0xf bank_mask:0xf\n\t"
      "v_mfma_f32_32x32x16_bf16 v[82:97], v[44:47], v[28:31], v[82:97]\n\t"
      "ds_read_b128 v[12:15], v3 offset:4096\n\t"
      "ds_read_b128 v[16:19], v3 offset:8192\n\t"
      "ds_read_b128 v[20:23], v3 offset:12288\n\t"
      "v_cvt_pk_bf16_f32 v40, v58, v59\n\t"
      "v_lshlrev_b32 v60, 16, v49\n\t"
      "v_and_b32 v61, s21, v49\n\t"
      "v_mul_f32_dpp v60, v57, v60 row_newbcast:2 row_mask:0xf bank_mask:0xf\n\t"
      "v_mfma_f32_32x32x16_bf16 v[98:113], v[44:47], v[32:35], v[98:113]\n\t"
      "v_mul_f32_dpp v61, v57, v61 row_newbcast:3 row_mask:0xf bank_mask:0xf\n\t"
      "v_cvt_pk_bf16_f32 v41, v60, v61\n\t"
      "v_lshlrev_b32 v62, 16, v50\n\t"
      "v_and_b32 v63, s21, v50\n\t"
      "v_mul_f32_dpp v62, v57, v62 row_newbcast:4 row_mask:0xf bank_mask:0xf\n\t"
      "v_mul_f32_dpp v63, v57, v63 row_newbcast:5 row_mask:0xf bank_mask:0xf\n\t"
      "v_add_u32 v6, 0x100, v6\n\t"
      "v_bfi_b32 v6, s23, v6, v7\n\t"
      "v_mfma_f32_32x32x16_bf16 v[114:129], v[44:47], v[36:39], v[114:129]\n\t"
      "v_cvt_pk_bf16_f32 v42, v62, v63\n\t"
      "v_lshlrev_b32 v64, 16, v51\n\t"
      "v_and_b32 v65, s21, v51\n\t"
      "v_mul_f32_dpp v64, v57, v64 row_newbcast:6 row_mask:0xf bank_mask:0xf\n\t"
      "v_mul_f32_dpp v65, v57, v65 row_newbcast:7 row_mask:0xf bank_mask:0xf\n\t"
      "v_cvt_pk_bf16_f32 v43, v64, v65\n\t"
      "ds_read_b32 v56, v6\n\t"
      "s_barrier\n\t"
      "s_waitcnt lgkmcnt(0)\n\t"
      "v_mfma_f32_32x32x16_bf16 v[66:81], v[40:43], v[8:11], v[66:81]\n\t"
      "ds_read_b64_tr_b16 v[48:49], v5 offset:32768\n\t"
      "ds_read_b64_tr_b16 v[50:51], v5 offset:33792\n\t"
      "ds_read_b128 v[24:27], v4 offset:0\n\t"
      "v_lshlrev_b32 v58, 16, v52\n\t"
      "v_and_b32 v59, s21, v52\n\t"
      "v_mul_f32_dpp v58, v57, v58 row_newbcast:8 row_mask:0xf bank_mask:0xf\n\t"
      "v_mul_f32_dpp v59, v57, v59 row_newbcast:9 row_mask:0xf bank_mask:0xf\n\t"
      "v_mfma_f32_32x32x16_bf16 v[82:97], v[40:43], v[12:15], v[82:97]\n\t"
      "ds_read_b128 v[28:31], v4 offset:4096\n\t"
      "ds_read_b128 v[32:35], v4 offset:8192\n\t"
      "ds_read_b128 v[36:39], v4 offset:12288\n\t"
      "v_cvt_pk_bf16_f32 v44, v58, v59\n\t"
      "v_lshlrev_b32 v60, 16, v53\n\t"
      "v_and_b32 v61, s21, v53\n\t"
      "v_mul_f32_dpp v60, v57, v60 row_newbcast:10 row_mask:0xf bank_mask:0xf\n\t"
      "v_mfma_f32_32x32x16_bf16 v[98:113], v[40:43], v[16:19], v[98:113]\n\t"
      "v_mul_f32_dpp v61, v57, v61 row_newbcast:11 row_mask:0xf bank_mask:0xf\n\t"
      "v_cvt_pk_bf16_f32 v45, v60, v61\n\t"
      "v_lshlrev_b32 v62, 16, v54\n\t"
      "v_and_b32 v63, s21, v54\n\t"
      "v_mul_f32_dpp v62, v57, v62 row_newbcast:12 row_mask:0xf bank_mask:0xf\n\t"
      "v_mul_f32_dpp v63, v57, v63 row_newbcast:13 row_mask:0xf bank_mask:0xf\n\t"
      "v_mfma_f32_32x32x16_bf16 v[114:129], v[40:43], v[20:23], v[114:129]\n\t"
      "v_cvt_pk_bf16_f32 v46, v62, v63\n\t"
      "v_lshlrev_b32 v64, 16, v55\n\t"
      "v_and_b32 v65, s21, v55\n\t"
      "v_mul_f32_dpp v64, v57, v64 row_newbcast:14 row_mask:0xf bank_mask:0xf\n\t"
      "v_mul_f32_dpp v65, v57, v65 row_newbcast:15 row_mask:0xf bank_mask:0xf\n\t"
      "v_cvt_pk_bf16_f32 v47, v64, v65\n\t"
      "s_nop 0\n\t"
      "s_waitcnt lgkmcnt(0)\n\t"
      "v_mfma_f32_32x32x16_bf16 v[66:81], v[44:47], v[24:27], v[66:81]\n\t"
      "ds_read_b64_tr_b16 v[52:53], v5 offset:36864\n\t"
      "ds_read_b64_tr_b16 v[54:55], v5 offset:37888\n\t"
      "ds_read_b128 v[8:11], v1 offset:32768\n\t"
      "v_lshlrev_b32 v58, 16, v48\n\t"
      "v_and_b32 v59, s21, v48\n\t"
      "v_mul_f32_dpp v58, v56, v58 row_newbcast:0 row_mask:0xf bank_mask:0xf\n\t"
      "v_mul_f32_dpp v59, v56, v59 row_newbcast:1 row_mask:0xf bank_mask:0xf\n\t"
      "v_mfma_f32_32x32x16_bf16 v[82:97], v[44:47], v[28:31], v[82:97]\n\t"
      "ds_read_b128 v[12:15], v1 offset:36864\n\t"
      "ds_read_b128 v[16:19], v1 offset:40960\n\t"
      "ds_read_b128 v[20:23], v1 offset:45056\n\t"
      "v_cvt_pk_bf16_f32 v40, v58, v59\n\t"
      "v_lshlrev_b32 v60, 16, v49\n\t"
      "v_and_b32 v61, s21, v49\n\t"
      "v_mul_f32_dpp v60, v56, v60 row_newbcast:2 row_mask:0xf bank_mask:0xf\n\t"
      "v_mfma_f32_32x32x16_bf16 v[98:113], v[44:47], v[32:35], v[98:113]\n\t"
      "v_mul_f32_dpp v61, v56, v61 row_newbcast:3 row_mask:0xf bank_mask:0xf\n\t"
      "v_cvt_pk_bf16_f32 v41, v60, v61\n\t"
      "v_lshlrev_b32 v62, 16, v50\n\t"
      "v_and_b32 v63, s21, v50\n\t"
      "v_mul_f32_dpp v62, v56, v62 row_newbcast:4 row_mask:0xf bank_mask:0xf\n\t"
      "v_mul_f32_dpp v63, v56, v63 row_newbcast:5 row_mask:0xf bank_mask:0xf\n\t"
      "v_mfma_f32_32x32x16_bf16 v[114:129], v[44:47], v[36:39], v[114:129]\n\t"
      "v_cvt_pk_bf16_f32 v42, v62, v63\n\t"
      "v_lshlrev_b32 v64, 16, v51\n\t"
      "v_and_b32 v65, s21, v51\n\t"
      "v_mul_f32_dpp v64, v56, v64 row_newbcast:6 row_mask:0xf bank_mask:0xf\n\t"
      "v_mul_f32_dpp v65, v56, v65 row_newbcast:7 row_mask:0xf bank_mask:0xf\n\t"
      "v_cvt_pk_bf16_f32 v43, v64, v65\n\t"
      "v_xor_b32 v1, s22, v1\n\t"
      "ds_read_b32 v57, v6 offset:128\n\t"
      "s_waitcnt lgkmcnt(0)\n\t"
      "v_mfma_f32_32x32x16_bf16 v[66:81], v[40:43], v[8:11], v[66:81]\n\t"
      "ds_read_b64_tr_b16 v[48:49], v5 offset:40960\n\t"
      "ds_read_b64_tr_b16 v[50:51], v5 offset:41984\n\t"
      "ds_read_b128 v[24:27], v2 offset:32768\n\t"
      "v_lshlrev_b32 v58, 16, v52\n\t"
      "v_and_b32 v59, s21, v52\n\t"
      "v_mul_f32_dpp v58, v56, v58 row_newbcast:8 row_mask:0xf bank_mask:0xf\n\t"
      "v_mul_f32_dpp v59, v56, v59 row_newbcast:9 row_mask:0xf bank_mask:0xf\n\t"
      "v_mfma_f32_32x32x16_bf16 v[82:97], v[40:43], v[12:15], v[82:97]\n\t"
      "ds_read_b128 v[28:31], v2 offset:36864\n\t"
      "ds_read_b128 v[32:35], v2 offset:40960\n\t"
      "ds_read_b128 v[36:39], v2 offset:45056\n\t"
      "v_cvt_pk_bf16_f32 v44, v58, v59\n\t"
      "v_lshlrev_b32 v60, 16, v53\n\t"
      "v_and_b32 v61, s21, v53\n\t"
      "v_mul_f32_dpp v60, v56, v60 row_newbcast:10 row_mask:0xf bank_mask:0xf\n\t"
      "v_mfma_f32_32x32x16_bf16 v[98:113], v[40:43], v[16:19], v[98:113]\n\t"
      "v_mul_f32_dpp v61, v56, v61 row_newbcast:11 row_mask:0xf bank_mask:0xf\n\t"
      "v_cvt_pk_bf16_f32 v45, v60, v61\n\t"
      "v_lshlrev_b32 v62, 16, v54\n\t"
      "v_and_b32 v63, s21, v54\n\t"
      "v_mul_f32_dpp v62, v56, v62 row_newbcast:12 row_mask:0xf bank_mask:0xf\n\t"
      "v_mul_f32_dpp v63, v56, v63 row_newbcast:13 row_mask:0xf bank_mask:0xf\n\t"
      "v_mfma_f32_32x32x16_bf16 v[114:129], v[40:43], v[20:23], v[114:129]\n\t"
      "v_cvt_pk_bf16_f32 v46, v62, v63\n\t"
      "v_lshlrev_b32 v64, 16, v55\n\t"
      "v_and_b32 v65, s21, v55\n\t"
      "v_mul_f32_dpp v64, v56, v64 row_newbcast:14 row_mask:0xf bank_mask:0xf\n\t"
      "v_mul_f32_dpp v65, v56, v65 row_newbcast:15 row_mask:0xf bank_mask:0xf\n\t"
      "v_cvt_pk_bf16_f32 v47, v64, v65\n\t"
      "v_xor_b32 v2, s22, v2\n\t"
      "s_waitcnt lgkmcnt(0)\n\t"
      "v_mfma_f32_32x32x16_bf16 v[66:81], v[44:47], v[24:27], v[66:81]\n\t"
      "ds_read_b64_tr_b16 v[52:53], v5 offset:45056\n\t"
      "ds_read_b64_tr_b16 v[54:55], v5 offset:46080\n\t"
      "ds_read_b128 v[8:11], v3 offset:32768\n\t"
      "v_lshlrev_b32 v58, 16, v48\n\t"
      "v_and_b32 v59, s21, v48\n\t"
      "v_mul_f32_dpp v58, v57, v58 row_newbcast:0 row_mask:0xf bank_mask:0xf\n\t"
      "v_mul_f32_dpp v59, v57, v59 row_newbcast:1 row_mask:0xf bank_mask:0xf\n\t"
      "v_mfma_f32_32x32x16_bf16 v[82:97], v[44:47], v[28:31], v[82:97]\n\t"
      "ds_read_b128 v[12:15], v3 offset:36864\n\t"
      "ds_read_b128 v[16:19], v3 offset:40960\n\t"
      "ds_read_b128 v[20:23], v3 offset:45056\n\t"
      "v_cvt_pk_bf16_f32 v40, v58, v59\n\t"
      "v_lshlrev_b32 v60, 16, v49\n\t"
      "v_and_b32 v61, s21, v49\n\t"
      "v_mul_f32_dpp v60, v57, v60 row_newbcast:2 row_mask:0xf bank_mask:0xf\n\t"
      "v_mfma_f32_32x32x16_bf16 v[98:113], v[44:47], v[32:35], v[98:113]\n\t"
      "v_mul_f32_dpp v61, v57, v61 row_newbcast:3 row_mask:0xf bank_mask:0xf\n\t"
      "v_cvt_pk_bf16_f32 v41, v60, v61\n\t"
      "v_lshlrev_b32 v62, 16, v50\n\t"
      "v_and_b32 v63, s21, v50\n\t"
      "v_mul_f32_dpp v62, v57, v62 row_newbcast:4 row_mask:0xf bank_mask:0xf\n\t"
      "v_mul_f32_dpp v63, v57, v63 row_newbcast:5 row_mask:0xf bank_mask:0xf\n\t"
      "v_add_u32 v6, 0x100, v6\n\t"
      "v_bfi_b32 v6, s23, v6, v7\n\t"
      "v_mfma_f32_32x32x16_bf16 v[114:129], v[44:47], v[36:39], v[114:129]\n\t"
      "v_cvt_pk_bf16_f32 v42, v62, v63\n\t"
      "v_lshlrev_b32 v64, 16, v51\n\t"
      "v_and_b32 v65, s21, v51\n\t"
      "v_mul_f32_dpp v64, v57, v64 row_newbcast:6 row_mask:0xf bank_mask:0xf\n\t"
      "v_mul_f32_dpp v65, v57, v65 row_newbcast:7 row_mask:0xf bank_mask:0xf\n\t"
      "v_cvt_pk_bf16_f32 v43, v64, v65\n\t"
      "v_xor_b32 v5, s22, v5\n\t"
      "v_xor_b32 v3, s22, v3\n\t"
      "ds_read_b32 v56, v6\n\t"
      "s_barrier\n\t"
      "s_waitcnt lgkmcnt(0)\n\t"
      "v_mfma_f32_32x32x16_bf16 v[66:81], v[40:43], v[8:11], v[66:81]\n\t"
      "ds_read_b64_tr_b16 v[48:49], v5 offset:0\n\t"
      "ds_read_b64_tr_b16 v[50:51], v5 offset:1024\n\t"
      "ds_read_b128 v[24:27], v4 offset:32768\n\t"
      "v_lshlrev_b32 v58, 16, v52\n\t"
      "v_and_b32 v59, s21, v52\n\t"
      "v_mul_f32_dpp v58, v57, v58 row_newbcast:8 row_mask:0xf bank_mask:0xf\n\t"
      "v_mul_f32_dpp v59, v57, v59 row_newbcast:9 row_mask:0xf bank_mask:0xf\n\t"
      "v_mfma_f32_32x32x16_bf16 v[82:97], v[40:43], v[12:15], v[82:97]\n\t"
      "ds_read_b128 v[28:31], v4 offset:36864\n\t"
      "ds_read_b128 v[32:35], v4 offset:40960\n\t"
      "ds_read_b128 v[36:39], v4 offset:45056\n\t"
      "v_cvt_pk_bf16_f32 v44, v58, v59\n\t"
      "v_lshlrev_b32 v60, 16, v53\n\t"
      "v_and_b32 v61, s21, v53\n\t"
      "v_mul_f32_dpp v60, v57, v60 row_newbcast:10 row_mask:0xf bank_mask:0xf\n\t"
      "v_mfma_f32_32x32x16_bf16 v[98:113], v[40:43], v[16:19], v[98:113]\n\t"
      "v_mul_f32_dpp v61, v57, v61 row_newbcast:11 row_mask:0xf bank_mask:0xf\n\t"
      "v_cvt_pk_bf16_f32 v45, v60, v61\n\t"
      "v_lshlrev_b32 v62, 16, v54\n\t"
      "v_and_b32 v63, s21, v54\n\t"
      "v_mul_f32_dpp v62, v57, v62 row_newbcast:12 row_mask:0xf bank_mask:0xf\n\t"
      "v_mul_f32_dpp v63, v57, v63 row_newbcast:13 row_mask:0xf bank_mask:0xf\n\t"
      "v_mfma_f32_32x32x16_bf16 v[114:129], v[40:43], v[20:23], v[114:129]\n\t"
      "v_cvt_pk_bf16_f32 v46, v62, v63\n\t"
      "v_lshlrev_b32 v64, 16, v55\n\t"
      "v_and_b32 v65, s21, v55\n\t"
      "v_mul_f32_dpp v64, v57, v64 row_newbcast:14 row_mask:0xf bank_mask:0xf\n\t"
      "v_mul_f32_dpp v65, v57, v65 row_newbcast:15 row_mask:0xf bank_mask:0xf\n\t"
      "v_cvt_pk_bf16_f32 v47, v64, v65\n\t"
      "v_xor_b32 v4, s22, v4\n\t"
      "s_waitcnt lgkmcnt(0)\n\t"
      "v_mfma_f32_32x32x16_bf16 v[66:81], v[44:47], v[24:27], v[66:81]\n\t"
      "ds_read_b64_tr_b16 v[52:53], v5 offset:4096\n\t"
      "ds_read_b64_tr_b16 v[54:55], v5 offset:5120\n\t"
      "ds_read_b128 v[8:11], v1 offset:0\n\t"
      "v_lshlrev_b32 v58, 16, v48\n\t"
      "v_and_b32 v59, s21, v48\n\t"
      "v_mul_f32_dpp v58, v56, v58 row_newbcast:0 row_mask:0xf bank_mask:0xf\n\t"
      "v_mul_f32_dpp v59, v56, v59 row_newbcast:1 row_mask:0xf bank_mask:0xf\n\t"
      "v_mfma_f32_32x32x16_bf16 v[82:97], v[44:47], v[28:31], v[82:97]\n\t"
      "ds_read_b128 v[12:15], v1 offset:4096\n\t"
      "ds_read_b128 v[16:19], v1 offset:8192\n\t"
      "ds_read_b128 v[20:23], v1 offset:12288\n\t"
      "v_cvt_pk_bf16_f32 v40, v58, v59\n\t"
      "v_lshlrev_b32 v60, 16, v49\n\t"
      "v_and_b32 v61, s21, v49\n\t"
      "v_mul_f32_dpp v60, v56, v60 row_newbcast:2 row_mask:0xf bank_mask:0xf\n\t"
      "v_mfma_f32_32x32x16_bf16 v[98:113], v[44:47], v[32:35], v[98:113]\n\t"
      "v_mul_f32_dpp v61, v56, v61 row_newbcast:3 row_mask:0xf bank_mask:0xf\n\t"
      "v_cvt_pk_bf16_f32 v41, v60, v61\n\t"
      "v_lshlrev_b32 v62, 16, v50\n\t"
      "v_and_b32 v63, s21, v50\n\t"
      "v_mul_f32_dpp v62, v56, v62 row_newbcast:4 row_mask:0xf bank_mask:0xf\n\t"
      "v_mul_f32_dpp v63, v56, v63 row_newbcast:5 row_mask:0xf bank_mask:0xf\n\t"
      "v_mfma_f32_32x32x16_bf16 v[114:129], v[44:47], v[36:39], v[114:129]\n\t"
      "v_cvt_pk_bf16_f32 v42, v62, v63\n\t"
      "v_lshlrev_b32 v64, 16, v51\n\t"
      "v_and_b32 v65, s21, v51\n\t"
      "v_mul_f32_dpp v64, v56, v64 row_newbcast:6 row_mask:0xf bank_mask:0xf\n\t"
      "v_mul_f32_dpp v65, v56, v65 row_newbcast:7 row_mask:0xf bank_mask:0xf\n\t"
      "v_cvt_pk_bf16_f32 v43, v64, v65\n\t"
      "ds_read_b32 v57, v6 offset:128\n\t"
      "s_sub_u32 s20, s20, 1\n\t"
      "s_cmp_lg_u32 s20, 0\n\t"
      "s_cbranch_scc1 1b\n\t"
      "s_waitcnt lgkmcnt(0)\n\t"
      "s_nop 15\n\t"
      : "={v[66:81]}"(acc0), "={v[82:97]}"(acc1), "={v[98:113]}"(acc2), "={v[114:129]}"(acc3),
        "+{v1}"(xa0), "+{v2}"(xa1), "+{v3}"(xa2), "+{v4}"(xa3), "+{v5}"(wa), "+{v6}"(ga)
      :
      : "memory", "scc", "s20", "s21", "s22", "s23", "v7","v8","v9","v10","v11","v12","v13","v14","v15","v16","v17","v18","v19","v20","v21","v22","v23","v24","v25","v26","v27","v28","v29","v30","v31","v32","v33","v34","v35","v36","v37","v38","v39","v40","v41","v42","v43","v44","v45","v46","v47","v48","v49","v50","v51","v52","v53","v54","v55","v56","v57","v58","v59","v60","v61","v62","v63","v64","v65");

    ST(2); CT(4);
    bar();
    f32x4 c1[4], c2[4];
#pragma unroll
    for (int qq = 0; qq < 4; ++qq) {
      const int n0 = wid * 32 + 8 * qq + 4 * h;
      c1[qq] = *(const f32x4*)(smem + OFF_C + n0 * 4);
      c2[qq] = *(const f32x4*)(smem + OFF_C + 512 + n0 * 4);
    }
    auto fin = [&](const f32x16& acc, int mt) {
      const int lr = mt * 32 + (lane & 31);
      const float2 st = *(const float2*)(smem + OFF_ST + lr * 8);
      u32 d[4][2];
#pragma unroll
      for (int qq = 0; qq < 4; ++qq) {
        float o[4];
#pragma unroll
        for (int e = 0; e < 4; ++e) o[e] = acc[4 * qq + e] * st.x + (c1[qq][e] * st.y + c2[qq][e]);
        d[qq][0] = cvt_pk(o[0], o[1]); d[qq][1] = cvt_pk(o[2], o[3]);
      }
#pragma unroll
      for (int qq = 0; qq < 4; qq += 2) {
        auto r0 = __builtin_amdgcn_permlane32_swap(d[qq][0], d[qq + 1][0], false, false);
        auto r1 = __builtin_amdgcn_permlane32_swap(d[qq][1], d[qq + 1][1], false, false);
        u32x4 v = {r0[0], r1[0], r0[1], r1[1]};
        const int ch = wid * 4 + qq + h;
        *(u32x4*)(smem + OFF_OUT + lr * 256 + ((ch ^ (lr & 7)) * 16)) = v;
      }
    };
    fin(acc0, 0); fin(acc1, 1); fin(acc2, 2); fin(acc3, 3);
    wait_lgkm0();
    ST(18);
  } else {
    const int lw = wq;
    const u32x4 xrsrc = mk_rsrc(x, MD * KD * 2), wrsrc = mk_rsrc(w, KD * ND * 2);
    const int rr = lane >> 3, dr = lane >> 4;
    const u32 xv0 = (u32)(((rp * 128 + lw * 32 + rr) * KD + 8 * ((lane & 7) ^ (rr >> 1))) * 2);
    const u32 xv1 = xv0 ^ 64;
    const u32 wv = (u32)(((lw * 16 + dr) * ND + cp * 128 + 8 * ((lane & 15) ^ (dr << 2))) * 2);
    auto issue = [&](int u) {
      const int kt = (u + rot) & 15;
      const u32 sx = (u32)(kt * 128), sw_ = (u32)(kt * 64 * ND * 2);
      const u32 dst = lds0 + (u & (NS - 1)) * SLOT + lw * 4096;
#pragma unroll
      for (int i = 0; i < 4; ++i) {
        dma16(wv, wrsrc, sw_ + i * 4 * ND * 2, dst + 16384 + i * 1024);
        dma16((i & 1) ? xv1 : xv0, xrsrc, sx + i * 8 * KD * 2, dst + i * 1024);
      }
    };
    issue(0);
    {
      const u32x4 trsrc = mk_rsrc(lw < 2 ? g : b, KD * 2);
      dma16(lane * 16, trsrc, (lw & 1) * 1024, lds0 + (lw < 2 ? OFF_G16 : OFF_B16) + (lw & 1) * 1024);
      dma16(lane * 16, trsrc, OOB, lds0 + OFF_Z16 + (lw & 1) * 1024);
    }
    issue(1); issue(2);
    const int jp = lane & 15, kg = lane >> 4;
    u32 seladdr = OFF_Z16;
    if ((jp & 1) == (kg & 1) && jp < 4) seladdr = (jp < 2 ? OFF_G16 : OFF_B16) + (kg >> 1) * 16;
    bf16x8 ones;
    { const short o = ((jp & 1) == (kg & 1) && jp < 2) ? (short)0x3f80 : (short)0; ones = (bf16x8){o, o, o, o, o, o, o, o}; }
    f32x4 hacc = {0.f, 0.f, 0.f, 0.f}, gr0 = {0.f, 0.f, 0.f, 0.f}, gr1 = {0.f, 0.f, 0.f, 0.f}, sumx = {0.f, 0.f, 0.f, 0.f};
    const u32 xrow = (lw * 32 + (lane & 31)) * 128;
    auto extras = [&](int u) {
      const int kt = (u + rot) & 15;
      const char* sl = smem + (u & (NS - 1)) * SLOT;
#pragma unroll
      for (int s = 0; s < 4; ++s) {
        const bf16x8 xf = *(const bf16x8*)(sl + xrow + (((2 * s + h) ^ xr) * 16));
        s16x4 lo = __builtin_amdgcn_ds_read_tr16_b64_v4i16((lds_s16x4_p)(sl + waddr + s * 4096));
        s16x4 hi = __builtin_amdgcn_ds_read_tr16_b64_v4i16((lds_s16x4_p)(sl + waddr + s * 4096 + 1024));
        const bf16x8 wf = __builtin_shufflevector(lo, hi, 0, 1, 2, 3, 4, 5, 6, 7);
        const bf16x8 sf = *(const bf16x8*)(smem + seladdr + kt * 128 + s * 32);
        gr0 = __builtin_amdgcn_mfma_f32_4x4x4bf16_1k(lo4(xf), lo4(xf), gr0, 0, 0, 0);
        gr1 = __builtin_amdgcn_mfma_f32_4x4x4bf16_1k(hi4(xf), hi4(xf), gr1, 0, 0, 0);
        sumx = __builtin_amdgcn_mfma_f32_16x16x32_bf16(xf, ones, sumx, 0, 0, 0);
        hacc = __builtin_amdgcn_mfma_f32_16x16x32_bf16(wf, sf, hacc, 0, 0, 0);
      }
    };
    wait_vm<16>();
    bar();
    ST(1);
#pragma unroll 1
    for (int u = 0; u < 16; ++u) {
      extras(u);
      if (u < 14) wait_vm<8>(); else wait_vm<0>();
      wait_lgkm0();
      bar();
      if (u < 13) issue(u + 3);
    }
    ST(2);
    if (jp < 4) *(f32x4*)(smem + OFF_C + (jp >> 1) * 512 + (lw * 32 + (jp & 1) * 16 + 4 * kg) * 4) = hacc;
    if (jp < 2) *(f32x4*)(smem + OFF_SX + (lw * 32 + jp * 16 + 4 * kg) * 4) = sumx;
    const f32x4 gs = gr0 + gr1;
    float sq = p4 == 0 ? gs[0] : p4 == 1 ? gs[1] : p4 == 2 ? gs[2] : gs[3];
    sq += __shfl_xor(sq, 32);
    wait_lgkm0();
    {
      const float mu = *(const float*)(smem + OFF_SX + (lw * 32 + (lane & 31)) * 4) * (1.f / KD);
      const float var = fmaxf(sq * (1.f / KD) - mu * mu, 0.f);
      const float rs = rsqrtf(var + 1e-5f);
      if (lane < 32) *(float2*)(smem + OFF_ST + (lw * 32 + lane) * 8) = make_float2(rs, -mu * rs);
    }
    wait_lgkm0();
    ST(17);
    bar();
    ST(18);
  }
  bar();
  {
    const __amdgpu_buffer_rsrc_t orsrc = __builtin_amdgcn_make_buffer_rsrc((void*)out, 0, MD * ND * 2, 0x00020000);
    const int r0 = wid * 16 + (lane >> 4), ch = lane & 15;
#pragma unroll
    for (int i = 0; i < 4; ++i) {
      const int r = r0 + 4 * i;
      const u32x4 v = *(const u32x4*)(smem + OFF_OUT + r * 256 + ((ch ^ (r & 7)) * 16));
      __builtin_amdgcn_raw_buffer_store_b128(v, orsrc, (u32)(((rp * 128 + r) * ND + cp * 128 + ch * 8) * 2), 0, STAUX);
    }
  }
}

extern "C" void kernel_launch(void* const* d_in, const int* in_sizes, int n_in,
                              void* d_out, int out_size, void* d_ws, size_t ws_size, hipStream_t stream) {
  const u16* x = (const u16*)d_in[0]; const u16* g = (const u16*)d_in[1];
  const u16* b = (const u16*)d_in[2]; const u16* w = (const u16*)d_in[3];
  (void)hipFuncSetAttribute((const void*)lnlin_kernel, hipFuncAttributeMaxDynamicSharedMemorySize, LDS_BYTES);
  lnlin_kernel<<<256, 512, LDS_BYTES, stream>>>(x, g, b, w, (u16*)d_out, (unsigned long long*)((char*)d_ws + (64u << 20)));
}
```

```cpp
#define ENC 0
#include <hip/hip_runtime.h>
#include <hip/hip_bf16.h>
typedef unsigned int u32;
typedef unsigned short u16;
using bf16x8 = __attribute__((ext_vector_type(8))) short;
using s16x4  = __attribute__((ext_vector_type(4))) short;
using f32x4  = __attribute__((ext_vector_type(4))) float;
using f32x16 = __attribute__((ext_vector_type(16))) float;
using u32x4  = __attribute__((ext_vector_type(4))) u32;
using u32x2  = __attribute__((ext_vector_type(2))) u32;
typedef __attribute__((address_space(3))) s16x4* lds_s16x4_p;

#define ROTMODE 6
#define STAUX 2

constexpr int MD = 4096, KD = 1024, ND = 1024;
constexpr int NS = 4;
constexpr int SLOT = 32768;
constexpr int OFF_G32 = NS * SLOT;
constexpr int OFF_G16 = OFF_G32 + 4096;
constexpr int OFF_B16 = OFF_G16 + 2048 + 64;
constexpr int OFF_Z16 = OFF_B16 + 2048 + 64;
constexpr int OFF_ST  = OFF_Z16 + 2048;
constexpr int OFF_C   = OFF_ST + 1024;
constexpr int OFF_SX  = OFF_C + 1024;
constexpr int OFF_STAMP = OFF_SX + 512;
constexpr int LDS_BYTES = OFF_STAMP + 512;
constexpr int OFF_OUT = SLOT;
constexpr u32 OOB = 0x40000000u;

template<int N> __device__ __forceinline__ void wait_vm() { asm volatile("s_waitcnt vmcnt(%0)" :: "i"(N) : "memory"); }
__device__ __forceinline__ void wait_lgkm0() { asm volatile("s_waitcnt lgkmcnt(0)" ::: "memory"); }
__device__ __forceinline__ void bar() { asm volatile("s_barrier" ::: "memory"); }
#define ST(i)
#define CT(i)
#define STDUMP()
__device__ __forceinline__ u32 cvt_pk(float lo, float hi) {
  typedef __attribute__((ext_vector_type(2))) float f2; typedef __attribute__((ext_vector_type(2))) __bf16 b2;
  f2 v = {lo, hi}; b2 r = __builtin_convertvector(v, b2); return __builtin_bit_cast(u32, r);
}
__device__ __forceinline__ u32x4 mk_rsrc(const void* p, u32 bytes) {
  u32x4 r; r[0] = (u32)(size_t)p; r[1] = (u32)((size_t)p >> 32) & 0xffffu; r[2] = bytes; r[3] = 0x00020000u; return r;
}
__device__ __forceinline__ void dma16(u32 voff, u32x4 rsrc, u32 soff, u32 dst) {
  u32 keep;
  asm volatile("s_mov_b32 %0, m0\n\ts_mov_b32 m0, %4\n\ts_nop 0\n\t"
               "buffer_load_dwordx4 %1, %2, %3 offen lds\n\ts_mov_b32 m0, %0"
               : "=&s"(keep) : "v"(voff), "s"(rsrc), "s"(soff), "s"(dst) : "memory");
}
__device__ __forceinline__ s16x4 lo4(bf16x8 v) { return __builtin_shufflevector(v, v, 0, 1, 2, 3); }
__device__ __forceinline__ s16x4 hi4(bf16x8 v) { return __builtin_shufflevector(v, v, 4, 5, 6, 7); }

__global__ __launch_bounds__(512, 2)
void lnlin_kernel(const u16* __restrict__ x, const u16* __restrict__ g, const u16* __restrict__ b,
                  const u16* __restrict__ w, u16* __restrict__ out, unsigned long long* stamps) {
  extern __shared__ __attribute__((aligned(16))) char smem[];
  const int bid = blockIdx.x, xcd = bid & 7, idx = bid >> 3;
  const int rp = xcd * 4 + (idx >> 3), cp = idx & 7;
  const int tid = threadIdx.x, lane = tid & 63;
  const int wid = __builtin_amdgcn_readfirstlane(tid >> 6);
  ST(0);
  const int rot = xcd * 2 + ((cp + (idx >> 3)) & 1) * 8;
  const u32 lds0 = (u32)(size_t)smem;
  const unsigned long long tstart_ = __builtin_amdgcn_s_memrealtime();
  unsigned long long enc_ = 0;
#define PADTO(T) do{ while (__builtin_amdgcn_s_memrealtime() - tstart_ < (T)) __builtin_amdgcn_s_sleep(1); }while(0)
  const int h = lane >> 5;
  const int xr = (lane >> 1) & 7;
  const int blk = (lane >> 4) & 1, q = (lane & 15) >> 2, p4 = lane & 3;
  const int wq = wid & 3;
  const u32 waddr = 16384 + (8 * h + q) * 256 + 16 * (4 * (wq ^ q) + 2 * blk + (p4 >> 1)) + 8 * (p4 & 1);

  if (wid < 4) {
    {
      const u32x2 gv = *(const u32x2*)(g + tid * 4);
      u32x4 t; t[0] = gv[0] << 16; t[1] = gv[0] & 0xffff0000u; t[2] = gv[1] << 16; t[3] = gv[1] & 0xffff0000u;
      *(u32x4*)(smem + OFF_G32 + tid * 16) = t;
    }
    u32 xa0 = lds0 + (lane & 31) * 128 + (((0 + h) ^ xr) * 16), xa1 = lds0 + (lane & 31) * 128 + (((2 + h) ^ xr) * 16);
    u32 xa2 = lds0 + (lane & 31) * 128 + (((4 + h) ^ xr) * 16), xa3 = lds0 + (lane & 31) * 128 + (((6 + h) ^ xr) * 16);
    u32 wa = lds0 + waddr;
    u32 ga = lds0 + OFF_G32 + (rot & 15) * 256 + (16 * ((lane & 15) >> 3) + 8 * h + (lane & 7)) * 4;
    f32x16 acc0, acc1, acc2, acc3;
    ST(1); CT(3);
    asm volatile(
      "s_mov_b32 s21, 0xffff0000\n\t"
      "s_mov_b32 s22, 0x10000\n\t"
      "s_mov_b32 s23, 0xfff\n\t"
      "s_mov_b32 s20, 8\n\t"
      "v_mov_b32 v7, v6\n\t"
      "v_mov_b32 v66, 0\n\t"
      "v_mov_b32 v67, 0\n\t"
      "v_mov_b32 v68, 0\n\t"
      "v_mov_b32 v69, 0\n\t"
      "v_mov_b32 v70, 0\n\t"
      "v_mov_b32 v71, 0\n\t"
      "v_mov_b32 v72, 0\n\t"
      "v_mov_b32 v73, 0\n\t"
      "v_mov_b32 v74, 0\n\t"
      "v_mov_b32 v75, 0\n\t"
      "v_mov_b32 v76, 0\n\t"
      "v_mov_b32 v77, 0\n\t"
      "v_mov_b32 v78, 0\n\t"
      "v_mov_b32 v79, 0\n\t"
      "v_mov_b32 v80, 0\n\t"
      "v_mov_b32 v81, 0\n\t"
      "v_mov_b32 v82, 0\n\t"
      "v_mov_b32 v83, 0\n\t"
      "v_mov_b32 v84, 0\n\t"
      "v_mov_b32 v85, 0\n\t"
      "v_mov_b32 v86, 0\n\t"
      "v_mov_b32 v87, 0\n\t"
      "v_mov_b32 v88, 0\n\t"
      "v_mov_b32 v89, 0\n\t"
      "v_mov_b32 v90, 0\n\t"
      "v_mov_b32 v91, 0\n\t"
      "v_mov_b32 v92, 0\n\t"
      "v_mov_b32 v93, 0\n\t"
      "v_mov_b32 v94, 0\n\t"
      "v_mov_b32 v95, 0\n\t"
      "v_mov_b32 v96, 0\n\t"
      "v_mov_b32 v97, 0\n\t"
      "v_mov_b32 v98, 0\n\t"
      "v_mov_b32 v99, 0\n\t"
      "v_mov_b32 v100, 0\n\t"
      "v_mov_b32 v101, 0\n\t"
      "v_mov_b32 v102, 0\n\t"
      "v_mov_b32 v103, 0\n\t"
      "v_mov_b32 v104, 0\n\t"
      "v_mov_b32 v105, 0\n\t"
      "v_mov_b32 v106, 0\n\t"
      "v_mov_b32 v107, 0\n\t"
      "v_mov_b32 v108, 0\n\t"
      "v_mov_b32 v109, 0\n\t"
      "v_mov_b32 v110, 0\n\t"
      "v_mov_b32 v111, 0\n\t"
      "v_mov_b32 v112, 0\n\t"
      "v_mov_b32 v113, 0\n\t"
      "v_mov_b32 v114, 0\n\t"
      "v_mov_b32 v115, 0\n\t"
      "v_mov_b32 v116, 0\n\t"
      "v_mov_b32 v117, 0\n\t"
      "v_mov_b32 v118, 0\n\t"
      "v_mov_b32 v119, 0\n\t"
      "v_mov_b32 v120, 0\n\t"
      "v_mov_b32 v121, 0\n\t"
      "v_mov_b32 v122, 0\n\t"
      "v_mov_b32 v123, 0\n\t"
      "v_mov_b32 v124, 0\n\t"
      "v_mov_b32 v125, 0\n\t"
      "v_mov_b32 v126, 0\n\t"
      "v_mov_b32 v127, 0\n\t"
      "v_mov_b32 v128, 0\n\t"
      "v_mov_b32 v129, 0\n\t"
      "s_waitcnt lgkmcnt(0)\n\t"
      "s_barrier\n\t"
      "ds_read_b64_tr_b16 v[48:49], v5 offset:0\n\t"
      "ds_read_b64_tr_b16 v[50:51], v5 offset:1024\n\t"
      "ds_read_b32 v56, v6\n\t"
      "ds_read_b128 v[8:11], v1 offset:0\n\t"
      "ds_read_b128 v[12:15], v1 offset:4096\n\t"
      "ds_read_b128 v[16:19], v1 offset:8192\n\t"
      "ds_read_b128 v[20:23], v1 offset:12288\n\t"
      "ds_read_b64_tr_b16 v[52:53], v5 offset:4096\n\t"
      "ds_read_b64_tr_b16 v[54:55], v5 offset:5120\n\t"
      "ds_read_b32 v57, v6 offset:128\n\t"
      "s_waitcnt lgkmcnt(7)\n\t"
      "v_lshlrev_b32 v58, 16, v48\n\t"
      "v_and_b32 v59, s21, v48\n\t"
      "v_mul_f32_dpp v58, v56, v58 row_newbcast:0 row_mask:0xf bank_mask:0xf\n\t"
      "v_mul_f32_dpp v59, v56, v59 row_newbcast:1 row_mask:0xf bank_mask:0xf\n\t"
      "v_cvt_pk_bf16_f32 v40, v58, v59\n\t"
      "v_lshlrev_b32 v60, 16, v49\n\t"
      "v_and_b32 v61, s21, v49\n\t"
      "v_mul_f32_dpp v60, v56, v60 row_newbcast:2 row_mask:0xf bank_mask:0xf\n\t"
      "v_mul_f32_dpp v61, v56, v61 row_newbcast:3 row_mask:0xf bank_mask:0xf\n\t"
      "v_cvt_pk_bf16_f32 v41, v60, v61\n\t"
      "v_lshlrev_b32 v62, 16, v50\n\t"
      "v_and_b32 v63, s21, v50\n\t"
      "v_mul_f32_dpp v62, v56, v62 row_newbcast:4 row_mask:0xf bank_mask:0xf\n\t"
      "v_mul_f32_dpp v63, v56, v63 row_newbcast:5 row_mask:0xf bank_mask:0xf\n\t"
      "v_cvt_pk_bf16_f32 v42, v62, v63\n\t"
      "v_lshlrev_b32 v64, 16, v51\n\t"
      "v_and_b32 v65, s21, v51\n\t"
      "v_mul_f32_dpp v64, v56, v64 row_newbcast:6 row_mask:0xf bank_mask:0xf\n\t"
      "v_mul_f32_dpp v65, v56, v65 row_newbcast:7 row_mask:0xf bank_mask:0xf\n\t"
      "v_cvt_pk_bf16_f32 v43, v64, v65\n\t"
      "s_nop 0\n\t"
      "1:\n\t"
      "s_waitcnt lgkmcnt(0)\n\t"
      "v_mfma_f32_32x32x16_bf16 v[66:81], v[40:43], v[8:11], v[66:81]\n\t"
      "ds_read_b64_tr_b16 v[48:49], v5 offset:8192\n\t"
      "ds_read_b64_tr_b16 v[50:51], v5 offset:9216\n\t"
      "ds_read_b128 v[24:27], v2 offset:0\n\t"
      "v_lshlrev_b32 v58, 16, v52\n\t"
      "v_and_b32 v59, s21, v52\n\t"
      "v_mul_f32_dpp v58, v56, v58 row_newbcast:8 row_mask:0xf bank_mask:0xf\n\t"
      "v_mul_f32_dpp v59, v56, v59 row_newbcast:9 row_mask:0xf bank_mask:0xf\n\t"
      "v_mfma_f32_32x32x16_bf16 v[82:97], v[40:43], v[12:15], v[82:97]\n\t"
      "ds_read_b128 v[28:31], v2 offset:4096\n\t"
      "ds_read_b128 v[32:35], v2 offset:8192\n\t"
      "ds_read_b128 v[36:39], v2 offset:12288\n\t"
      "v_cvt_pk_bf16_f32 v44, v58, v59\n\t"
      "v_lshlrev_b32 v60, 16, v53\n\t"
      "v_and_b32 v61, s21, v53\n\t"
      "v_mul_f32_dpp v60, v56, v60 row_newbcast:10 row_mask:0xf bank_mask:0xf\n\t"
      "v_mfma_f32_32x32x16_bf16 v[98:113], v[40:43], v[16:19], v[98:113]\n\t"
      "v_mul_f32_dpp v61, v56, v61 row_newbcast:11 row_mask:0xf bank_mask:0xf\n\t"
      "v_cvt_pk_bf16_f32 v45, v60, v61\n\t"
      "v_lshlrev_b32 v62, 16, v54\n\t"
      "v_and_b32 v63, s21, v54\n\t"
      "v_mul_f32_dpp v62, v56, v62 row_newbcast:12 row_mask:0xf bank_mask:0xf\n\t"
      "v_mul_f32_dpp v63, v56, v63 row_newbcast:13 row_mask:0xf bank_mask:0xf\n\t"
      "v_mfma_f32_32x32x16_bf16 v[114:129], v[40:43], v[20:23], v[114:129]\n\t"
      "v_cvt_pk_bf16_f32 v46, v62, v63\n\t"
      "v_lshlrev_b32 v64, 16, v55\n\t"
      "v_and_b32 v65, s21, v55\n\t"
      "v_mul_f32_dpp v64, v56, v64 row_newbcast:14 row_mask:0xf bank_mask:0xf\n\t"
      "v_mul_f32_dpp v65, v56, v65 row_newbcast:15 row_mask:0xf bank_mask:0xf\n\t"
      "v_cvt_pk_bf16_f32 v47, v64, v65\n\t"
      "s_nop 0\n\t"
      "s_waitcnt lgkmcnt(0)\n\t"
      "v_mfma_f32_32x32x16_bf16 v[66:81], v[44:47], v[24:27], v[66:81]\n\t"
      "ds_read_b64_tr_b16 v[52:53], v5 offset:12288\n\t"
      "ds_read_b64_tr_b16 v[54:55], v5 offset:13312\n\t"
      "ds_read_b128 v[8:11], v3 offset:0\n\t"
      "v_lshlrev_b32 v58, 16, v48\n\t"
      "v_and_b32 v59, s21, v48\n\t"
      "v_mul_f32_dpp v58, v57, v58 row_newbcast:0 row_mask:0xf bank_mask:0xf\n\t"
      "v_mul_f32_dpp v59, v57, v59 row_newbcast:1 row_mask:0xf bank_mask:0xf\n\t"
      "v_mfma_f32_32x32x16_bf16 v[82:97], v[44:47], v[28:31], v[82:97]\n\t"
      "ds_read_b128 v[12:15], v3 offset:4096\n\t"
      "ds_read_b128 v[16:19], v3 offset:8192\n\t"
      "ds_read_b128 v[20:23], v3 offset:12288\n\t"
      "v_cvt_pk_bf16_f32 v40, v58, v59\n\t"
      "v_lshlrev_b32 v60, 16, v49\n\t"
      "v_and_b32 v61, s21, v49\n\t"
      "v_mul_f32_dpp v60, v57, v60 row_newbcast:2 row_mask:0xf bank_mask:0xf\n\t"
      "v_mfma_f32_32x32x16_bf16 v[98:113], v[44:47], v[32:35], v[98:113]\n\t"
      "v_mul_f32_dpp v61, v57, v61 row_newbcast:3 row_mask:0xf bank_mask:0xf\n\t"
      "v_cvt_pk_bf16_f32 v41, v60, v61\n\t"
      "v_lshlrev_b32 v62, 16, v50\n\t"
      "v_and_b32 v63, s21, v50\n\t"
      "v_mul_f32_dpp v62, v57, v62 row_newbcast:4 row_mask:0xf bank_mask:0xf\n\t"
      "v_mul_f32_dpp v63, v57, v63 row_newbcast:5 row_mask:0xf bank_mask:0xf\n\t"
      "v_add_u32 v6, 0x100, v6\n\t"
      "v_bfi_b32 v6, s23, v6, v7\n\t"
      "v_mfma_f32_32x32x16_bf16 v[114:129], v[44:47], v[36:39], v[114:129]\n\t"
      "v_cvt_pk_bf16_f32 v42, v62, v63\n\t"
      "v_lshlrev_b32 v64, 16, v51\n\t"
      "v_and_b32 v65, s21, v51\n\t"
      "v_mul_f32_dpp v64, v57, v64 row_newbcast:6 row_mask:0xf bank_mask:0xf\n\t"
      "v_mul_f32_dpp v65, v57, v65 row_newbcast:7 row_mask:0xf bank_mask:0xf\n\t"
      "v_cvt_pk_bf16_f32 v43, v64, v65\n\t"
      "ds_read_b32 v56, v6\n\t"
      "s_barrier\n\t"
      "s_waitcnt lgkmcnt(0)\n\t"
      "v_mfma_f32_32x32x16_bf16 v[66:81], v[40:43], v[8:11], v[66:81]\n\t"
      "ds_read_b64_tr_b16 v[48:49], v5 offset:32768\n\t"
      "ds_read_b64_tr_b16 v[50:51], v5 offset:33792\n\t"
      "ds_read_b128 v[24:27], v4 offset:0\n\t"
      "v_lshlrev_b32 v58, 16, v52\n\t"
      "v_and_b32 v59, s21, v52\n\t"
      "v_mul_f32_dpp v58, v57, v58 row_newbcast:8 row_mask:0xf bank_mask:0xf\n\t"
      "v_mul_f32_dpp v59, v57, v59 row_newbcast:9 row_mask:0xf bank_mask:0xf\n\t"
      "v_mfma_f32_32x32x16_bf16 v[82:97], v[40:43], v[12:15], v[82:97]\n\t"
      "ds_read_b128 v[28:31], v4 offset:4096\n\t"
      "ds_read_b128 v[32:35], v4 offset:8192\n\t"
      "ds_read_b128 v[36:39], v4 offset:12288\n\t"
      "v_cvt_pk_bf16_f32 v44, v58, v59\n\t"
      "v_lshlrev_b32 v60, 16, v53\n\t"
      "v_and_b32 v61, s21, v53\n\t"
      "v_mul_f32_dpp v60, v57, v60 row_newbcast:10 row_mask:0xf bank_mask:0xf\n\t"
      "v_mfma_f32_32x32x16_bf16 v[98:113], v[40:43], v[16:19], v[98:113]\n\t"
      "v_mul_f32_dpp v61, v57, v61 row_newbcast:11 row_mask:0xf bank_mask:0xf\n\t"
      "v_cvt_pk_bf16_f32 v45, v60, v61\n\t"
      "v_lshlrev_b32 v62, 16, v54\n\t"
      "v_and_b32 v63, s21, v54\n\t"
      "v_mul_f32_dpp v62, v57, v62 row_newbcast:12 row_mask:0xf bank_mask:0xf\n\t"
      "v_mul_f32_dpp v63, v57, v63 row_newbcast:13 row_mask:0xf bank_mask:0xf\n\t"
      "v_mfma_f32_32x32x16_bf16 v[114:129], v[40:43], v[20:23], v[114:129]\n\t"
      "v_cvt_pk_bf16_f32 v46, v62, v63\n\t"
      "v_lshlrev_b32 v64, 16, v55\n\t"
      "v_and_b32 v65, s21, v55\n\t"
      "v_mul_f32_dpp v64, v57, v64 row_newbcast:14 row_mask:0xf bank_mask:0xf\n\t"
      "v_mul_f32_dpp v65, v57, v65 row_newbcast:15 row_mask:0xf bank_mask:0xf\n\t"
      "v_cvt_pk_bf16_f32 v47, v64, v65\n\t"
      "s_nop 0\n\t"
      "s_waitcnt lgkmcnt(0)\n\t"
      "v_mfma_f32_32x32x16_bf16 v[66:81], v[44:47], v[24:27], v[66:81]\n\t"
      "ds_read_b64_tr_b16 v[52:53], v5 offset:36864\n\t"
      "ds_read_b64_tr_b16 v[54:55], v5 offset:37888\n\t"
      "ds_read_b128 v[8:11], v1 offset:32768\n\t"
      "v_lshlrev_b32 v58, 16, v48\n\t"
      "v_and_b32 v59, s21, v48\n\t"
      "v_mul_f32_dpp v58, v56, v58 row_newbcast:0 row_mask:0xf bank_mask:0xf\n\t"
      "v_mul_f32_dpp v59, v56, v59 row_newbcast:1 row_mask:0xf bank_mask:0xf\n\t"
      "v_mfma_f32_32x32x16_bf16 v[82:97], v[44:47], v[28:31], v[82:97]\n\t"
      "ds_read_b128 v[12:15], v1 offset:36864\n\t"
      "ds_read_b128 v[16:19], v1 offset:40960\n\t"
      "ds_read_b128 v[20:23], v1 offset:45056\n\t"
      "v_cvt_pk_bf16_f32 v40, v58, v59\n\t"
      "v_lshlrev_b32 v60, 16, v49\n\t"
      "v_and_b32 v61, s21, v49\n\t"
      "v_mul_f32_dpp v60, v56, v60 row_newbcast:2 row_mask:0xf bank_mask:0xf\n\t"
      "v_mfma_f32_32x32x16_bf16 v[98:113], v[44:47], v[32:35], v[98:113]\n\t"
      "v_mul_f32_dpp v61, v56, v61 row_newbcast:3 row_mask:0xf bank_mask:0xf\n\t"
      "v_cvt_pk_bf16_f32 v41, v60, v61\n\t"
      "v_lshlrev_b32 v62, 16, v50\n\t"
      "v_and_b32 v63, s21, v50\n\t"
      "v_mul_f32_dpp v62, v56, v62 row_newbcast:4 row_mask:0xf bank_mask:0xf\n\t"
      "v_mul_f32_dpp v63, v56, v63 row_newbcast:5 row_mask:0xf bank_mask:0xf\n\t"
      "v_mfma_f32_32x32x16_bf16 v[114:129], v[44:47], v[36:39], v[114:129]\n\t"
      "v_cvt_pk_bf16_f32 v42, v62, v63\n\t"
      "v_lshlrev_b32 v64, 16, v51\n\t"
      "v_and_b32 v65, s21, v51\n\t"
      "v_mul_f32_dpp v64, v56, v64 row_newbcast:6 row_mask:0xf bank_mask:0xf\n\t"
      "v_mul_f32_dpp v65, v56, v65 row_newbcast:7 row_mask:0xf bank_mask:0xf\n\t"
      "v_cvt_pk_bf16_f32 v43, v64, v65\n\t"
      "v_xor_b32 v1, s22, v1\n\t"
      "ds_read_b32 v57, v6 offset:128\n\t"
      "s_waitcnt lgkmcnt(0)\n\t"
      "v_mfma_f32_32x32x16_bf16 v[66:81], v[40:43], v[8:11], v[66:81]\n\t"
      "ds_read_b64_tr_b16 v[48:49], v5 offset:40960\n\t"
      "ds_read_b64_tr_b16 v[50:51], v5 offset:41984\n\t"
      "ds_read_b128 v[24:27], v2 offset:32768\n\t"
      "v_lshlrev_b32 v58, 16, v52\n\t"
      "v_and_b32 v59, s21, v52\n\t"
      "v_mul_f32_dpp v58, v56, v58 row_newbcast:8 row_mask:0xf bank_mask:0xf\n\t"
      "v_mul_f32_dpp v59, v56, v59 row_newbcast:9 row_mask:0xf bank_mask:0xf\n\t"
      "v_mfma_f32_32x32x16_bf16 v[82:97], v[40:43], v[12:15], v[82:97]\n\t"
      "ds_read_b128 v[28:31], v2 offset:36864\n\t"
      "ds_read_b128 v[32:35], v2 offset:40960\n\t"
      "ds_read_b128 v[36:39], v2 offset:45056\n\t"
      "v_cvt_pk_bf16_f32 v44, v58, v59\n\t"
      "v_lshlrev_b32 v60, 16, v53\n\t"
      "v_and_b32 v61, s21, v53\n\t"
      "v_mul_f32_dpp v60, v56, v60 row_newbcast:10 row_mask:0xf bank_mask:0xf\n\t"
      "v_mfma_f32_32x32x16_bf16 v[98:113], v[40:43], v[16:19], v[98:113]\n\t"
      "v_mul_f32_dpp v61, v56, v61 row_newbcast:11 row_mask:0xf bank_mask:0xf\n\t"
      "v_cvt_pk_bf16_f32 v45, v60, v61\n\t"
      "v_lshlrev_b32 v62, 16, v54\n\t"
      "v_and_b32 v63, s21, v54\n\t"
      "v_mul_f32_dpp v62, v56, v62 row_newbcast:12 row_mask:0xf bank_mask:0xf\n\t"
      "v_mul_f32_dpp v63, v56, v63 row_newbcast:13 row_mask:0xf bank_mask:0xf\n\t"
      "v_mfma_f32_32x32x16_bf16 v[114:129], v[40:43], v[20:23], v[114:129]\n\t"
      "v_cvt_pk_bf16_f32 v46, v62, v63\n\t"
      "v_lshlrev_b32 v64, 16, v55\n\t"
      "v_and_b32 v65, s21, v55\n\t"
      "v_mul_f32_dpp v64, v56, v64 row_newbcast:14 row_mask:0xf bank_mask:0xf\n\t"
      "v_mul_f32_dpp v65, v56, v65 row_newbcast:15 row_mask:0xf bank_mask:0xf\n\t"
      "v_cvt_pk_bf16_f32 v47, v64, v65\n\t"
      "v_xor_b32 v2, s22, v2\n\t"
      "s_waitcnt lgkmcnt(0)\n\t"
      "v_mfma_f32_32x32x16_bf16 v[66:81], v[44:47], v[24:27], v[66:81]\n\t"
      "ds_read_b64_tr_b16 v[52:53], v5 offset:45056\n\t"
      "ds_read_b64_tr_b16 v[54:55], v5 offset:46080\n\t"
      "ds_read_b128 v[8:11], v3 offset:32768\n\t"
      "v_lshlrev_b32 v58, 16, v48\n\t"
      "v_and_b32 v59, s21, v48\n\t"
      "v_mul_f32_dpp v58, v57, v58 row_newbcast:0 row_mask:0xf bank_mask:0xf\n\t"
      "v_mul_f32_dpp v59, v57, v59 row_newbcast:1 row_mask:0xf bank_mask:0xf\n\t"
      "v_mfma_f32_32x32x16_bf16 v[82:97], v[44:47], v[28:31], v[82:97]\n\t"
      "ds_read_b128 v[12:15], v3 offset:36864\n\t"
      "ds_read_b128 v[16:19], v3 offset:40960\n\t"
      "ds_read_b128 v[20:23], v3 offset:45056\n\t"
      "v_cvt_pk_bf16_f32 v40, v58, v59\n\t"
      "v_lshlrev_b32 v60, 16, v49\n\t"
      "v_and_b32 v61, s21, v49\n\t"
      "v_mul_f32_dpp v60, v57, v60 row_newbcast:2 row_mask:0xf bank_mask:0xf\n\t"
      "v_mfma_f32_32x32x16_bf16 v[98:113], v[44:47], v[32:35], v[98:113]\n\t"
      "v_mul_f32_dpp v61, v57, v61 row_newbcast:3 row_mask:0xf bank_mask:0xf\n\t"
      "v_cvt_pk_bf16_f32 v41, v60, v61\n\t"
      "v_lshlrev_b32 v62, 16, v50\n\t"
      "v_and_b32 v63, s21, v50\n\t"
      "v_mul_f32_dpp v62, v57, v62 row_newbcast:4 row_mask:0xf bank_mask:0xf\n\t"
      "v_mul_f32_dpp v63, v57, v63 row_newbcast:5 row_mask:0xf bank_mask:0xf\n\t"
      "v_add_u32 v6, 0x100, v6\n\t"
      "v_bfi_b32 v6, s23, v6, v7\n\t"
      "v_mfma_f32_32x32x16_bf16 v[114:129], v[44:47], v[36:39], v[114:129]\n\t"
      "v_cvt_pk_bf16_f32 v42, v62, v63\n\t"
      "v_lshlrev_b32 v64, 16, v51\n\t"
      "v_and_b32 v65, s21, v51\n\t"
      "v_mul_f32_dpp v64, v57, v64 row_newbcast:6 row_mask:0xf bank_mask:0xf\n\t"
      "v_mul_f32_dpp v65, v57, v65 row_newbcast:7 row_mask:0xf bank_mask:0xf\n\t"
      "v_cvt_pk_bf16_f32 v43, v64, v65\n\t"
      "v_xor_b32 v5, s22, v5\n\t"
      "v_xor_b32 v3, s22, v3\n\t"
      "ds_read_b32 v56, v6\n\t"
      "s_barrier\n\t"
      "s_waitcnt lgkmcnt(0)\n\t"
      "v_mfma_f32_32x32x16_bf16 v[66:81], v[40:43], v[8:11], v[66:81]\n\t"
      "ds_read_b64_tr_b16 v[48:49], v5 offset:0\n\t"
      "ds_read_b64_tr_b16 v[50:51], v5 offset:1024\n\t"
      "ds_read_b128 v[24:27], v4 offset:32768\n\t"
      "v_lshlrev_b32 v58, 16, v52\n\t"
      "v_and_b32 v59, s21, v52\n\t"
      "v_mul_f32_dpp v58, v57, v58 row_newbcast:8 row_mask:0xf bank_mask:0xf\n\t"
      "v_mul_f32_dpp v59, v57, v59 row_newbcast:9 row_mask:0xf bank_mask:0xf\n\t"
      "v_mfma_f32_32x32x16_bf16 v[82:97], v[40:43], v[12:15], v[82:97]\n\t"
      "ds_read_b128 v[28:31], v4 offset:36864\n\t"
      "ds_read_b128 v[32:35], v4 offset:40960\n\t"
      "ds_read_b128 v[36:39], v4 offset:45056\n\t"
      "v_cvt_pk_bf16_f32 v44, v58, v59\n\t"
      "v_lshlrev_b32 v60, 16, v53\n\t"
      "v_and_b32 v61, s21, v53\n\t"
      "v_mul_f32_dpp v60, v57, v60 row_newbcast:10 row_mask:0xf bank_mask:0xf\n\t"
      "v_mfma_f32_32x32x16_bf16 v[98:113], v[40:43], v[16:19], v[98:113]\n\t"
      "v_mul_f32_dpp v61, v57, v61 row_newbcast:11 row_mask:0xf bank_mask:0xf\n\t"
      "v_cvt_pk_bf16_f32 v45, v60, v61\n\t"
      "v_lshlrev_b32 v62, 16, v54\n\t"
      "v_and_b32 v63, s21, v54\n\t"
      "v_mul_f32_dpp v62, v57, v62 row_newbcast:12 row_mask:0xf bank_mask:0xf\n\t"
      "v_mul_f32_dpp v63, v57, v63 row_newbcast:13 row_mask:0xf bank_mask:0xf\n\t"
      "v_mfma_f32_32x32x16_bf16 v[114:129], v[40:43], v[20:23], v[114:129]\n\t"
      "v_cvt_pk_bf16_f32 v46, v62, v63\n\t"
      "v_lshlrev_b32 v64, 16, v55\n\t"
      "v_and_b32 v65, s21, v55\n\t"
      "v_mul_f32_dpp v64, v57, v64 row_newbcast:14 row_mask:0xf bank_mask:0xf\n\t"
      "v_mul_f32_dpp v65, v57, v65 row_newbcast:15 row_mask:0xf bank_mask:0xf\n\t"
      "v_cvt_pk_bf16_f32 v47, v64, v65\n\t"
      "v_xor_b32 v4, s22, v4\n\t"
      "s_waitcnt lgkmcnt(0)\n\t"
      "v_mfma_f32_32x32x16_bf16 v[66:81], v[44:47], v[24:27], v[66:81]\n\t"
      "ds_read_b64_tr_b16 v[52:53], v5 offset:4096\n\t"
      "ds_read_b64_tr_b16 v[54:55], v5 offset:5120\n\t"
      "ds_read_b128 v[8:11], v1 offset:0\n\t"
      "v_lshlrev_b32 v58, 16, v48\n\t"
      "v_and_b32 v59, s21, v48\n\t"
      "v_mul_f32_dpp v58, v56, v58 row_newbcast:0 row_mask:0xf bank_mask:0xf\n\t"
      "v_mul_f32_dpp v59, v56, v59 row_newbcast:1 row_mask:0xf bank_mask:0xf\n\t"
      "v_mfma_f32_32x32x16_bf16 v[82:97], v[44:47], v[28:31], v[82:97]\n\t"
      "ds_read_b128 v[12:15], v1 offset:4096\n\t"
      "ds_read_b128 v[16:19], v1 offset:8192\n\t"
      "ds_read_b128 v[20:23], v1 offset:12288\n\t"
      "v_cvt_pk_bf16_f32 v40, v58, v59\n\t"
      "v_lshlrev_b32 v60, 16, v49\n\t"
      "v_and_b32 v61, s21, v49\n\t"
      "v_mul_f32_dpp v60, v56, v60 row_newbcast:2 row_mask:0xf bank_mask:0xf\n\t"
      "v_mfma_f32_32x32x16_bf16 v[98:113], v[44:47], v[32:35], v[98:113]\n\t"
      "v_mul_f32_dpp v61, v56, v61 row_newbcast:3 row_mask:0xf bank_mask:0xf\n\t"
      "v_cvt_pk_bf16_f32 v41, v60, v61\n\t"
      "v_lshlrev_b32 v62, 16, v50\n\t"
      "v_and_b32 v63, s21, v50\n\t"
      "v_mul_f32_dpp v62, v56, v62 row_newbcast:4 row_mask:0xf bank_mask:0xf\n\t"
      "v_mul_f32_dpp v63, v56, v63 row_newbcast:5 row_mask:0xf bank_mask:0xf\n\t"
      "v_mfma_f32_32x32x16_bf16 v[114:129], v[44:47], v[36:39], v[114:129]\n\t"
      "v_cvt_pk_bf16_f32 v42, v62, v63\n\t"
      "v_lshlrev_b32 v64, 16, v51\n\t"
      "v_and_b32 v65, s21, v51\n\t"
      "v_mul_f32_dpp v64, v56, v64 row_newbcast:6 row_mask:0xf bank_mask:0xf\n\t"
      "v_mul_f32_dpp v65, v56, v65 row_newbcast:7 row_mask:0xf bank_mask:0xf\n\t"
      "v_cvt_pk_bf16_f32 v43, v64, v65\n\t"
      "ds_read_b32 v57, v6 offset:128\n\t"
      "s_sub_u32 s20, s20, 1\n\t"
      "s_cmp_lg_u32 s20, 0\n\t"
      "s_cbranch_scc1 1b\n\t"
      "s_waitcnt lgkmcnt(0)\n\t"
      "s_nop 15\n\t"
      : "={v[66:81]}"(acc0), "={v[82:97]}"(acc1), "={v[98:113]}"(acc2), "={v[114:129]}"(acc3),
        "+{v1}"(xa0), "+{v2}"(xa1), "+{v3}"(xa2), "+{v4}"(xa3), "+{v5}"(wa), "+{v6}"(ga)
      :
      : "memory", "scc", "s20", "s21", "s22", "s23", "v7","v8","v9","v10","v11","v12","v13","v14","v15","v16","v17","v18","v19","v20","v21","v22","v23","v24","v25","v26","v27","v28","v29","v30","v31","v32","v33","v34","v35","v36","v37","v38","v39","v40","v41","v42","v43","v44","v45","v46","v47","v48","v49","v50","v51","v52","v53","v54","v55","v56","v57","v58","v59","v60","v61","v62","v63","v64","v65");

    ST(2); CT(4);
    bar();
    f32x4 c1[4], c2[4];
#pragma unroll
    for (int qq = 0; qq < 4; ++qq) {
      const int n0 = wid * 32 + 8 * qq + 4 * h;
      c1[qq] = *(const f32x4*)(smem + OFF_C + n0 * 4);
      c2[qq] = *(const f32x4*)(smem + OFF_C + 512 + n0 * 4);
    }
    auto fin = [&](const f32x16& acc, int mt) {
      const int lr = mt * 32 + (lane & 31);
      const float2 st = *(const float2*)(smem + OFF_ST + lr * 8);
      u32 d[4][2];
#pragma unroll
      for (int qq = 0; qq < 4; ++qq) {
        float o[4];
#pragma unroll
        for (int e = 0; e < 4; ++e) o[e] = acc[4 * qq + e] * st.x + (c1[qq][e] * st.y + c2[qq][e]);
        d[qq][0] = cvt_pk(o[0], o[1]); d[qq][1] = cvt_pk(o[2], o[3]);
      }
#pragma unroll
      for (int qq = 0; qq < 4; qq += 2) {
        auto r0 = __builtin_amdgcn_permlane32_swap(d[qq][0], d[qq + 1][0], false, false);
        auto r1 = __builtin_amdgcn_permlane32_swap(d[qq][1], d[qq + 1][1], false, false);
        u32x4 v = {r0[0], r1[0], r0[1], r1[1]};
        const int ch = wid * 4 + qq + h;
        *(u32x4*)(smem + OFF_OUT + lr * 256 + ((ch ^ (lr & 7)) * 16)) = v;
      }
    };
    fin(acc0, 0); fin(acc1, 1); fin(acc2, 2); fin(acc3, 3);
    wait_lgkm0();
    ST(18);
  } else {
    const int lw = wq;
    const u32x4 xrsrc = mk_rsrc(x, MD * KD * 2), wrsrc = mk_rsrc(w, KD * ND * 2);
    const int rr = lane >> 3, dr = lane >> 4;
    const u32 xv0 = (u32)(((rp * 128 + lw * 32 + rr) * KD + 8 * ((lane & 7) ^ (rr >> 1))) * 2);
    const u32 xv1 = xv0 ^ 64;
    const u32 wv = (u32)(((lw * 16 + dr) * ND + cp * 128 + 8 * ((lane & 15) ^ (dr << 2))) * 2);
    auto issue = [&](int u) {
      const int kt = (u + rot) & 15;
      const u32 sx = (u32)(kt * 128), sw_ = (u32)(kt * 64 * ND * 2);
      const u32 dst = lds0 + (u & (NS - 1)) * SLOT + lw * 4096;
#pragma unroll
      for (int i = 0; i < 4; ++i) {
        dma16(wv, wrsrc, sw_ + i * 4 * ND * 2, dst + 16384 + i * 1024);
        dma16((i & 1) ? xv1 : xv0, xrsrc, sx + i * 8 * KD * 2, dst + i * 1024);
      }
    };
    issue(0);
    {
      const u32x4 trsrc = mk_rsrc(lw < 2 ? g : b, KD * 2);
      dma16(lane * 16, trsrc, (lw & 1) * 1024, lds0 + (lw < 2 ? OFF_G16 : OFF_B16) + (lw & 1) * 1024);
      dma16(lane * 16, trsrc, OOB, lds0 + OFF_Z16 + (lw & 1) * 1024);
    }
    issue(1); issue(2);
    const int jp = lane & 15, kg = lane >> 4;
    u32 seladdr = OFF_Z16;
    if ((jp & 1) == (kg & 1) && jp < 4) seladdr = (jp < 2 ? OFF_G16 : OFF_B16) + (kg >> 1) * 16;
    bf16x8 ones;
    { const short o = ((jp & 1) == (kg & 1) && jp < 2) ? (short)0x3f80 : (short)0; ones = (bf16x8){o, o, o, o, o, o, o, o}; }
    f32x4 hacc = {0.f, 0.f, 0.f, 0.f}, gr0 = {0.f, 0.f, 0.f, 0.f}, gr1 = {0.f, 0.f, 0.f, 0.f}, sumx = {0.f, 0.f, 0.f, 0.f};
    const u32 xrow = (lw * 32 + (lane & 31)) * 128;
    auto extras = [&](int u) {
      const int kt = (u + rot) & 15;
      const char* sl = smem + (u & (NS - 1)) * SLOT;
#pragma unroll
      for (int s = 0; s < 4; ++s) {
        const bf16x8 xf = *(const bf16x8*)(sl + xrow + (((2 * s + h) ^ xr) * 16));
        s16x4 lo = __builtin_amdgcn_ds_read_tr16_b64_v4i16((lds_s16x4_p)(sl + waddr + s * 4096));
        s16x4 hi = __builtin_amdgcn_ds_read_tr16_b64_v4i16((lds_s16x4_p)(sl + waddr + s * 4096 + 1024));
        const bf16x8 wf = __builtin_shufflevector(lo, hi, 0, 1, 2, 3, 4, 5, 6, 7);
        const bf16x8 sf = *(const bf16x8*)(smem + seladdr + kt * 128 + s * 32);
        gr0 = __builtin_amdgcn_mfma_f32_4x4x4bf16_1k(lo4(xf), lo4(xf), gr0, 0, 0, 0);
        gr1 = __builtin_amdgcn_mfma_f32_4x4x4bf16_1k(hi4(xf), hi4(xf), gr1, 0, 0, 0);
        sumx = __builtin_amdgcn_mfma_f32_16x16x32_bf16(xf, ones, sumx, 0, 0, 0);
        hacc = __builtin_amdgcn_mfma_f32_16x16x32_bf16(wf, sf, hacc, 0, 0, 0);
      }
    };
    wait_vm<16>();
    bar();
    const unsigned long long r1_ = __builtin_amdgcn_s_memrealtime(), c1_ = __builtin_amdgcn_s_memtime();
    __builtin_amdgcn_s_waitcnt(0xC07F);
    ST(1);
#pragma unroll 1
    for (int u = 0; u < 16; ++u) {
      extras(u);
      if (u < 14) wait_vm<8>(); else wait_vm<0>();
      wait_lgkm0();
      bar();
      if (u < 13) issue(u + 3);
    }
    { const unsigned long long r2_ = __builtin_amdgcn_s_memrealtime(), c2_ = __builtin_amdgcn_s_memtime();
      enc_ = ENC == 0 ? 0 : ENC == 1 ? (r2_ - r1_) : ENC == 2 ? (c2_ - c1_) / 10 : ENC == 3 ? (r1_ - tstart_) : 0; }
    ST(2);
    if (jp < 4) *(f32x4*)(smem + OFF_C + (jp >> 1) * 512 + (lw * 32 + (jp & 1) * 16 + 4 * kg) * 4) = hacc;
    if (jp < 2) *(f32x4*)(smem + OFF_SX + (lw * 32 + jp * 16 + 4 * kg) * 4) = sumx;
    const f32x4 gs = gr0 + gr1;
    float sq = p4 == 0 ? gs[0] : p4 == 1 ? gs[1] : p4 == 2 ? gs[2] : gs[3];
    sq += __shfl_xor(sq, 32);
    wait_lgkm0();
    {
      const float mu = *(const float*)(smem + OFF_SX + (lw * 32 + (lane & 31)) * 4) * (1.f / KD);
      const float var = fmaxf(sq * (1.f / KD) - mu * mu, 0.f);
      const float rs = rsqrtf(var + 1e-5f);
      if (lane < 32) *(float2*)(smem + OFF_ST + (lw * 32 + lane) * 8) = make_float2(rs, -mu * rs);
    }
    wait_lgkm0();
    ST(17);
    bar();
    ST(18);
  }
  bar();
  {
    const __amdgpu_buffer_rsrc_t orsrc = __builtin_amdgcn_make_buffer_rsrc((void*)out, 0, MD * ND * 2, 0x00020000);
    const int r0 = wid * 16 + (lane >> 4), ch = lane & 15;
#pragma unroll
    for (int i = 0; i < 4; ++i) {
      const int r = r0 + 4 * i;
      const u32x4 v = *(const u32x4*)(smem + OFF_OUT + r * 256 + ((ch ^ (r & 7)) * 16));
      __builtin_amdgcn_raw_buffer_store_b128(v, orsrc, (u32)(((rp * 128 + r) * ND + cp * 128 + ch * 8) * 2), 0, STAUX);
    }
  }
  wait_vm<0>(); if (ENC == 4) enc_ = __builtin_amdgcn_s_memrealtime() - tstart_;
  PADTO(3000 + enc_);
}

extern "C" void kernel_launch(void* const* d_in, const int* in_sizes, int n_in,
                              void* d_out, int out_size, void* d_ws, size_t ws_size, hipStream_t stream) {
  const u16* x = (const u16*)d_in[0]; const u16* g = (const u16*)d_in[1];
  const u16* b = (const u16*)d_in[2]; const u16* w = (const u16*)d_in[3];
  (void)hipFuncSetAttribute((const void*)lnlin_kernel, hipFuncAttributeMaxDynamicSharedMemorySize, LDS_BYTES);
  lnlin_kernel<<<256, 512, LDS_BYTES, stream>>>(x, g, b, w, (u16*)d_out, (unsigned long long*)((char*)d_ws + (64u << 20)));
}
```

```cpp
#define ENC 0
#define SYN 0
#define SYNREP 1
#include <hip/hip_runtime.h>
#include <hip/hip_bf16.h>
typedef unsigned int u32;
typedef unsigned short u16;
using bf16x8 = __attribute__((ext_vector_type(8))) short;
using s16x4  = __attribute__((ext_vector_type(4))) short;
using f32x4  = __attribute__((ext_vector_type(4))) float;
using f32x16 = __attribute__((ext_vector_type(16))) float;
using u32x4  = __attribute__((ext_vector_type(4))) u32;
using u32x2  = __attribute__((ext_vector_type(2))) u32;
typedef __attribute__((address_space(3))) s16x4* lds_s16x4_p;

#define ROTMODE 6
#define STAUX 2

constexpr int MD = 4096, KD = 1024, ND = 1024;
constexpr int NS = 4;
constexpr int SLOT = 32768;
constexpr int OFF_G32 = NS * SLOT;
constexpr int OFF_G16 = OFF_G32 + 4096;
constexpr int OFF_B16 = OFF_G16 + 2048 + 64;
constexpr int OFF_Z16 = OFF_B16 + 2048 + 64;
constexpr int OFF_ST  = OFF_Z16 + 2048;
constexpr int OFF_C   = OFF_ST + 1024;
constexpr int OFF_SX  = OFF_C + 1024;
constexpr int OFF_STAMP = OFF_SX + 512;
constexpr int LDS_BYTES = OFF_STAMP + 512;
constexpr int OFF_OUT = SLOT;
constexpr u32 OOB = 0x40000000u;

template<int N> __device__ __forceinline__ void wait_vm() { asm volatile("s_waitcnt vmcnt(%0)" :: "i"(N) : "memory"); }
__device__ __forceinline__ void wait_lgkm0() { asm volatile("s_waitcnt lgkmcnt(0)" ::: "memory"); }
__device__ __forceinline__ void bar() { asm volatile("s_barrier" ::: "memory"); }
#define ST(i)
#define CT(i)
#define STDUMP()
__device__ __forceinline__ u32 cvt_pk(float lo, float hi) {
  typedef __attribute__((ext_vector_type(2))) float f2; typedef __attribute__((ext_vector_type(2))) __bf16 b2;
  f2 v = {lo, hi}; b2 r = __builtin_convertvector(v, b2); return __builtin_bit_cast(u32, r);
}
__device__ __forceinline__ u32x4 mk_rsrc(const void* p, u32 bytes) {
  u32x4 r; r[0] = (u32)(size_t)p; r[1] = (u32)((size_t)p >> 32) & 0xffffu; r[2] = bytes; r[3] = 0x00020000u; return r;
}
__device__ __forceinline__ void dma16(u32 voff, u32x4 rsrc, u32 soff, u32 dst) {
  u32 keep;
  asm volatile("s_mov_b32 %0, m0\n\ts_mov_b32 m0, %4\n\ts_nop 0\n\t"
               "buffer_load_dwordx4 %1, %2, %3 offen lds\n\ts_mov_b32 m0, %0"
               : "=&s"(keep) : "v"(voff), "s"(rsrc), "s"(soff), "s"(dst) : "memory");
}
__device__ __forceinline__ s16x4 lo4(bf16x8 v) { return __builtin_shufflevector(v, v, 0, 1, 2, 3); }
__device__ __forceinline__ s16x4 hi4(bf16x8 v) { return __builtin_shufflevector(v, v, 4, 5, 6, 7); }

__global__ __launch_bounds__(512, 2)
void lnlin_kernel(const u16* __restrict__ x, const u16* __restrict__ g, const u16* __restrict__ b,
                  const u16* __restrict__ w, u16* __restrict__ out, unsigned long long* stamps) {
  extern __shared__ __attribute__((aligned(16))) char smem[];
  const int bid = blockIdx.x, xcd = bid & 7, idx = bid >> 3;
  const int rp = xcd * 4 + (idx >> 3), cp = idx & 7;
  const int tid = threadIdx.x, lane = tid & 63;
  const int wid = __builtin_amdgcn_readfirstlane(tid >> 6);
  ST(0);
  const int rot = xcd * 2 + ((cp + (idx >> 3)) & 1) * 8;
  const u32 lds0 = (u32)(size_t)smem;
  const unsigned long long tstart_ = __builtin_amdgcn_s_memrealtime();
  unsigned long long enc_ = 0;
#define PADTO(T) do{ while (__builtin_amdgcn_s_memrealtime() - tstart_ < (T)) __builtin_amdgcn_s_sleep(1); }while(0)
  const int h = lane >> 5;
  const int xr = (lane >> 1) & 7;
  const int blk = (lane >> 4) & 1, q = (lane & 15) >> 2, p4 = lane & 3;
  const int wq = wid & 3;
  const u32 waddr = 16384 + (8 * h + q) * 256 + 16 * (4 * (wq ^ q) + 2 * blk + (p4 >> 1)) + 8 * (p4 & 1);

  if (wid < 4) {
    if (SYN) {
      bf16x8 fa[4], fb[4];
#pragma unroll
      for (int i = 0; i < 4; ++i) { fa[i] = *(const bf16x8*)(x + (size_t)(bid * 16 + i) * 1024 + tid * 8 % 1024); fb[i] = *(const bf16x8*)(w + (size_t)(bid * 4 + i) * 1024 + tid * 8 % 1024); }
      f32x16 a0 = {}, a1 = {}, a2 = {}, a3 = {};
      f32x4 q0 = {}, q1 = {}, q2 = {}, q3 = {}, q4 = {}, q5 = {}, q6 = {}, q7 = {};
      asm volatile("" : "+v"(fa[0]), "+v"(fa[1]), "+v"(fa[2]), "+v"(fa[3]), "+v"(fb[0]), "+v"(fb[1]), "+v"(fb[2]), "+v"(fb[3]));
      const unsigned long long r1_ = __builtin_amdgcn_s_memrealtime(), c1_ = __builtin_amdgcn_s_memtime();
      __builtin_amdgcn_s_waitcnt(0xC07F);
#pragma unroll 1
      for (int it = 0; it < 16 * SYNREP; ++it) {
#pragma unroll
        for (int j = 0; j < 4; ++j) {
          if (SYN == 1) {
            a0 = __builtin_amdgcn_mfma_f32_32x32x16_bf16(fb[j], fa[0], a0, 0, 0, 0);
            a1 = __builtin_amdgcn_mfma_f32_32x32x16_bf16(fb[j], fa[1], a1, 0, 0, 0);
            a2 = __builtin_amdgcn_mfma_f32_32x32x16_bf16(fb[j], fa[2], a2, 0, 0, 0);
            a3 = __builtin_amdgcn_mfma_f32_32x32x16_bf16(fb[j], fa[3], a3, 0, 0, 0);
          } else if (SYN == 2) {
            q0 = __builtin_amdgcn_mfma_f32_16x16x32_bf16(fb[j], fa[0], q0, 0, 0, 0);
            q1 = __builtin_amdgcn_mfma_f32_16x16x32_bf16(fb[j], fa[1], q1, 0, 0, 0);
            q2 = __builtin_amdgcn_mfma_f32_16x16x32_bf16(fb[j], fa[2], q2, 0, 0, 0);
            q3 = __builtin_amdgcn_mfma_f32_16x16x32_bf16(fb[j], fa[3], q3, 0, 0, 0);
            q4 = __builtin_amdgcn_mfma_f32_16x16x32_bf16(fb[(j + 1) & 3], fa[0], q4, 0, 0, 0);
            q5 = __builtin_amdgcn_mfma_f32_16x16x32_bf16(fb[(j + 1) & 3], fa[1], q5, 0, 0, 0);
            q6 = __builtin_amdgcn_mfma_f32_16x16x32_bf16(fb[(j + 1) & 3], fa[2], q6, 0, 0, 0);
            q7 = __builtin_amdgcn_mfma_f32_16x16x32_bf16(fb[(j + 1) & 3], fa[3], q7, 0, 0, 0);
          }
        }
      }
      asm volatile("s_nop 15" :: "v"(a0), "v"(a1), "v"(a2), "v"(a3), "v"(q0), "v"(q1), "v"(q2), "v"(q3), "v"(q4), "v"(q5), "v"(q6), "v"(q7));
      const unsigned long long r2_ = __builtin_amdgcn_s_memrealtime(), c2_ = __builtin_amdgcn_s_memtime();
      enc_ = ENC == 5 ? (r2_ - r1_) : (c2_ - c1_) / 10;
    }
    {
      const u32x2 gv = *(const u32x2*)(g + tid * 4);
      u32x4 t; t[0] = gv[0] << 16; t[1] = gv[0] & 0xffff0000u; t[2] = gv[1] << 16; t[3] = gv[1] & 0xffff0000u;
      *(u32x4*)(smem + OFF_G32 + tid * 16) = t;
    }
    u32 xa0 = lds0 + (lane & 31) * 128 + (((0 + h) ^ xr) * 16), xa1 = lds0 + (lane & 31) * 128 + (((2 + h) ^ xr) * 16);
    u32 xa2 = lds0 + (lane & 31) * 128 + (((4 + h) ^ xr) * 16), xa3 = lds0 + (lane & 31) * 128 + (((6 + h) ^ xr) * 16);
    u32 wa = lds0 + waddr;
    u32 ga = lds0 + OFF_G32 + (rot & 15) * 256 + (16 * ((lane & 15) >> 3) + 8 * h + (lane & 7)) * 4;
    f32x16 acc0, acc1, acc2, acc3;
    ST(1); CT(3);
    asm volatile(
      "s_mov_b32 s21, 0xffff0000\n\t"
      "s_mov_b32 s22, 0x10000\n\t"
      "s_mov_b32 s23, 0xfff\n\t"
      "s_mov_b32 s20, 8\n\t"
      "v_mov_b32 v7, v6\n\t"
      "v_mov_b32 v66, 0\n\t"
      "v_mov_b32 v67, 0\n\t"
      "v_mov_b32 v68, 0\n\t"
      "v_mov_b32 v69, 0\n\t"
      "v_mov_b32 v70, 0\n\t"
      "v_mov_b32 v71, 0\n\t"
      "v_mov_b32 v72, 0\n\t"
      "v_mov_b32 v73, 0\n\t"
      "v_mov_b32 v74, 0\n\t"
      "v_mov_b32 v75, 0\n\t"
      "v_mov_b32 v76, 0\n\t"
      "v_mov_b32 v77, 0\n\t"
      "v_mov_b32 v78, 0\n\t"
      "v_mov_b32 v79, 0\n\t"
      "v_mov_b32 v80, 0\n\t"
      "v_mov_b32 v81, 0\n\t"
      "v_mov_b32 v82, 0\n\t"
      "v_mov_b32 v83, 0\n\t"
      "v_mov_b32 v84, 0\n\t"
      "v_mov_b32 v85, 0\n\t"
      "v_mov_b32 v86, 0\n\t"
      "v_mov_b32 v87, 0\n\t"
      "v_mov_b32 v88, 0\n\t"
      "v_mov_b32 v89, 0\n\t"
      "v_mov_b32 v90, 0\n\t"
      "v_mov_b32 v91, 0\n\t"
      "v_mov_b32 v92, 0\n\t"
      "v_mov_b32 v93, 0\n\t"
      "v_mov_b32 v94, 0\n\t"
      "v_mov_b32 v95, 0\n\t"
      "v_mov_b32 v96, 0\n\t"
      "v_mov_b32 v97, 0\n\t"
      "v_mov_b32 v98, 0\n\t"
      "v_mov_b32 v99, 0\n\t"
      "v_mov_b32 v100, 0\n\t"
      "v_mov_b32 v101, 0\n\t"
      "v_mov_b32 v102, 0\n\t"
      "v_mov_b32 v103, 0\n\t"
      "v_mov_b32 v104, 0\n\t"
      "v_mov_b32 v105, 0\n\t"
      "v_mov_b32 v106, 0\n\t"
      "v_mov_b32 v107, 0\n\t"
      "v_mov_b32 v108, 0\n\t"
      "v_mov_b32 v109, 0\n\t"
      "v_mov_b32 v110, 0\n\t"
      "v_mov_b32 v111, 0\n\t"
      "v_mov_b32 v112, 0\n\t"
      "v_mov_b32 v113, 0\n\t"
      "v_mov_b32 v114, 0\n\t"
      "v_mov_b32 v115, 0\n\t"
      "v_mov_b32 v116, 0\n\t"
      "v_mov_b32 v117, 0\n\t"
      "v_mov_b32 v118, 0\n\t"
      "v_mov_b32 v119, 0\n\t"
      "v_mov_b32 v120, 0\n\t"
      "v_mov_b32 v121, 0\n\t"
      "v_mov_b32 v122, 0\n\t"
      "v_mov_b32 v123, 0\n\t"
      "v_mov_b32 v124, 0\n\t"
      "v_mov_b32 v125, 0\n\t"
      "v_mov_b32 v126, 0\n\t"
      "v_mov_b32 v127, 0\n\t"
      "v_mov_b32 v128, 0\n\t"
      "v_mov_b32 v129, 0\n\t"
      "s_waitcnt lgkmcnt(0)\n\t"
      "s_barrier\n\t"
      "ds_read_b64_tr_b16 v[48:49], v5 offset:0\n\t"
      "ds_read_b64_tr_b16 v[50:51], v5 offset:1024\n\t"
      "ds_read_b32 v56, v6\n\t"
      "ds_read_b128 v[8:11], v1 offset:0\n\t"
      "ds_read_b128 v[12:15], v1 offset:4096\n\t"
      "ds_read_b128 v[16:19], v1 offset:8192\n\t"
      "ds_read_b128 v[20:23], v1 offset:12288\n\t"
      "ds_read_b64_tr_b16 v[52:53], v5 offset:4096\n\t"
      "ds_read_b64_tr_b16 v[54:55], v5 offset:5120\n\t"
      "ds_read_b32 v57, v6 offset:128\n\t"
      "s_waitcnt lgkmcnt(7)\n\t"
      "v_lshlrev_b32 v58, 16, v48\n\t"
      "v_and_b32 v59, s21, v48\n\t"
      "v_mul_f32_dpp v58, v56, v58 row_newbcast:0 row_mask:0xf bank_mask:0xf\n\t"
      "v_mul_f32_dpp v59, v56, v59 row_newbcast:1 row_mask:0xf bank_mask:0xf\n\t"
      "v_cvt_pk_bf16_f32 v40, v58, v59\n\t"
      "v_lshlrev_b32 v60, 16, v49\n\t"
      "v_and_b32 v61, s21, v49\n\t"
      "v_mul_f32_dpp v60, v56, v60 row_newbcast:2 row_mask:0xf bank_mask:0xf\n\t"
      "v_mul_f32_dpp v61, v56, v61 row_newbcast:3 row_mask:0xf bank_mask:0xf\n\t"
      "v_cvt_pk_bf16_f32 v41, v60, v61\n\t"
      "v_lshlrev_b32 v62, 16, v50\n\t"
      "v_and_b32 v63, s21, v50\n\t"
      "v_mul_f32_dpp v62, v56, v62 row_newbcast:4 row_mask:0xf bank_mask:0xf\n\t"
      "v_mul_f32_dpp v63, v56, v63 row_newbcast:5 row_mask:0xf bank_mask:0xf\n\t"
      "v_cvt_pk_bf16_f32 v42, v62, v63\n\t"
      "v_lshlrev_b32 v64, 16, v51\n\t"
      "v_and_b32 v65, s21, v51\n\t"
      "v_mul_f32_dpp v64, v56, v64 row_newbcast:6 row_mask:0xf bank_mask:0xf\n\t"
      "v_mul_f32_dpp v65, v56, v65 row_newbcast:7 row_mask:0xf bank_mask:0xf\n\t"
      "v_cvt_pk_bf16_f32 v43, v64, v65\n\t"
      "s_nop 0\n\t"
      "1:\n\t"
      "s_waitcnt lgkmcnt(0)\n\t"
      "v_mfma_f32_32x32x16_bf16 v[66:81], v[40:43], v[8:11], v[66:81]\n\t"
      "ds_read_b64_tr_b16 v[48:49], v5 offset:8192\n\t"
      "ds_read_b64_tr_b16 v[50:51], v5 offset:9216\n\t"
      "ds_read_b128 v[24:27], v2 offset:0\n\t"
      "v_lshlrev_b32 v58, 16, v52\n\t"
      "v_and_b32 v59, s21, v52\n\t"
      "v_mul_f32_dpp v58, v56, v58 row_newbcast:8 row_mask:0xf bank_mask:0xf\n\t"
      "v_mul_f32_dpp v59, v56, v59 row_newbcast:9 row_mask:0xf bank_mask:0xf\n\t"
      "v_mfma_f32_32x32x16_bf16 v[82:97], v[40:43], v[12:15], v[82:97]\n\t"
      "ds_read_b128 v[28:31], v2 offset:4096\n\t"
      "ds_read_b128 v[32:35], v2 offset:8192\n\t"
      "ds_read_b128 v[36:39], v2 offset:12288\n\t"
      "v_cvt_pk_bf16_f32 v44, v58, v59\n\t"
      "v_lshlrev_b32 v60, 16, v53\n\t"
      "v_and_b32 v61, s21, v53\n\t"
      "v_mul_f32_dpp v60, v56, v60 row_newbcast:10 row_mask:0xf bank_mask:0xf\n\t"
      "v_mfma_f32_32x32x16_bf16 v[98:113], v[40:43], v[16:19], v[98:113]\n\t"
      "v_mul_f32_dpp v61, v56, v61 row_newbcast:11 row_mask:0xf bank_mask:0xf\n\t"
      "v_cvt_pk_bf16_f32 v45, v60, v61\n\t"
      "v_lshlrev_b32 v62, 16, v54\n\t"
      "v_and_b32 v63, s21, v54\n\t"
      "v_mul_f32_dpp v62, v56, v62 row_newbcast:12 row_mask:0xf bank_mask:0xf\n\t"
      "v_mul_f32_dpp v63, v56, v63 row_newbcast:13 row_mask:0xf bank_mask:0xf\n\t"
      "v_mfma_f32_32x32x16_bf16 v[114:129], v[40:43], v[20:23], v[114:129]\n\t"
      "v_cvt_pk_bf16_f32 v46, v62, v63\n\t"
      "v_lshlrev_b32 v64, 16, v55\n\t"
      "v_and_b32 v65, s21, v55\n\t"
      "v_mul_f32_dpp v64, v56, v64 row_newbcast:14 row_mask:0xf bank_mask:0xf\n\t"
      "v_mul_f32_dpp v65, v56, v65 row_newbcast:15 row_mask:0xf bank_mask:0xf\n\t"
      "v_cvt_pk_bf16_f32 v47, v64, v65\n\t"
      "s_nop 0\n\t"
      "s_waitcnt lgkmcnt(0)\n\t"
      "v_mfma_f32_32x32x16_bf16 v[66:81], v[44:47], v[24:27], v[66:81]\n\t"
      "ds_read_b64_tr_b16 v[52:53], v5 offset:12288\n\t"
      "ds_read_b64_tr_b16 v[54:55], v5 offset:13312\n\t"
      "ds_read_b128 v[8:11], v3 offset:0\n\t"
      "v_lshlrev_b32 v58, 16, v48\n\t"
      "v_and_b32 v59, s21, v48\n\t"
      "v_mul_f32_dpp v58, v57, v58 row_newbcast:0 row_mask:0xf bank_mask:0xf\n\t"
      "v_mul_f32_dpp v59, v57, v59 row_newbcast:1 row_mask:0xf bank_mask:0xf\n\t"
      "v_mfma_f32_32x32x16_bf16 v[82:97], v[44:47], v[28:31], v[82:97]\n\t"
      "ds_read_b128 v[12:15], v3 offset:4096\n\t"
      "ds_read_b128 v[16:19], v3 offset:8192\n\t"
      "ds_read_b128 v[20:23], v3 offset:12288\n\t"
      "v_cvt_pk_bf16_f32 v40, v58, v59\n\t"
      "v_lshlrev_b32 v60, 16, v49\n\t"
      "v_and_b32 v61, s21, v49\n\t"
      "v_mul_f32_dpp v60, v57, v60 row_newbcast:2 row_mask:0xf bank_mask:0xf\n\t"
      "v_mfma_f32_32x32x16_bf16 v[98:113], v[44:47], v[32:35], v[98:113]\n\t"
      "v_mul_f32_dpp v61, v57, v61 row_newbcast:3 row_mask:0xf bank_mask:0xf\n\t"
      "v_cvt_pk_bf16_f32 v41, v60, v61\n\t"
      "v_lshlrev_b32 v62, 16, v50\n\t"
      "v_and_b32 v63, s21, v50\n\t"
      "v_mul_f32_dpp v62, v57, v62 row_newbcast:4 row_mask:0xf bank_mask:0xf\n\t"
      "v_mul_f32_dpp v63, v57, v63 row_newbcast:5 row_mask:0xf bank_mask:0xf\n\t"
      "v_add_u32 v6, 0x100, v6\n\t"
      "v_bfi_b32 v6, s23, v6, v7\n\t"
      "v_mfma_f32_32x32x16_bf16 v[114:129], v[44:47], v[36:39], v[114:129]\n\t"
      "v_cvt_pk_bf16_f32 v42, v62, v63\n\t"
      "v_lshlrev_b32 v64, 16, v51\n\t"
      "v_and_b32 v65, s21, v51\n\t"
      "v_mul_f32_dpp v64, v57, v64 row_newbcast:6 row_mask:0xf bank_mask:0xf\n\t"
      "v_mul_f32_dpp v65, v57, v65 row_newbcast:7 row_mask:0xf bank_mask:0xf\n\t"
      "v_cvt_pk_bf16_f32 v43, v64, v65\n\t"
      "ds_read_b32 v56, v6\n\t"
      "s_barrier\n\t"
      "s_waitcnt lgkmcnt(0)\n\t"
      "v_mfma_f32_32x32x16_bf16 v[66:81], v[40:43], v[8:11], v[66:81]\n\t"
      "ds_read_b64_tr_b16 v[48:49], v5 offset:32768\n\t"
      "ds_read_b64_tr_b16 v[50:51], v5 offset:33792\n\t"
      "ds_read_b128 v[24:27], v4 offset:0\n\t"
      "v_lshlrev_b32 v58, 16, v52\n\t"
      "v_and_b32 v59, s21, v52\n\t"
      "v_mul_f32_dpp v58, v57, v58 row_newbcast:8 row_mask:0xf bank_mask:0xf\n\t"
      "v_mul_f32_dpp v59, v57, v59 row_newbcast:9 row_mask:0xf bank_mask:0xf\n\t"
      "v_mfma_f32_32x32x16_bf16 v[82:97], v[40:43], v[12:15], v[82:97]\n\t"
      "ds_read_b128 v[28:31], v4 offset:4096\n\t"
      "ds_read_b128 v[32:35], v4 offset:8192\n\t"
      "ds_read_b128 v[36:39], v4 offset:12288\n\t"
      "v_cvt_pk_bf16_f32 v44, v58, v59\n\t"
      "v_lshlrev_b32 v60, 16, v53\n\t"
      "v_and_b32 v61, s21, v53\n\t"
      "v_mul_f32_dpp v60, v57, v60 row_newbcast:10 row_mask:0xf bank_mask:0xf\n\t"
      "v_mfma_f32_32x32x16_bf16 v[98:113], v[40:43], v[16:19], v[98:113]\n\t"
      "v_mul_f32_dpp v61, v57, v61 row_newbcast:11 row_mask:0xf bank_mask:0xf\n\t"
      "v_cvt_pk_bf16_f32 v45, v60, v61\n\t"
      "v_lshlrev_b32 v62, 16, v54\n\t"
      "v_and_b32 v63, s21, v54\n\t"
      "v_mul_f32_dpp v62, v57, v62 row_newbcast:12 row_mask:0xf bank_mask:0xf\n\t"
      "v_mul_f32_dpp v63, v57, v63 row_newbcast:13 row_mask:0xf bank_mask:0xf\n\t"
      "v_mfma_f32_32x32x16_bf16 v[114:129], v[40:43], v[20:23], v[114:129]\n\t"
      "v_cvt_pk_bf16_f32 v46, v62, v63\n\t"
      "v_lshlrev_b32 v64, 16, v55\n\t"
      "v_and_b32 v65, s21, v55\n\t"
      "v_mul_f32_dpp v64, v57, v64 row_newbcast:14 row_mask:0xf bank_mask:0xf\n\t"
      "v_mul_f32_dpp v65, v57, v65 row_newbcast:15 row_mask:0xf bank_mask:0xf\n\t"
      "v_cvt_pk_bf16_f32 v47, v64, v65\n\t"
      "s_nop 0\n\t"
      "s_waitcnt lgkmcnt(0)\n\t"
      "v_mfma_f32_32x32x16_bf16 v[66:81], v[44:47], v[24:27], v[66:81]\n\t"
      "ds_read_b64_tr_b16 v[52:53], v5 offset:36864\n\t"
      "ds_read_b64_tr_b16 v[54:55], v5 offset:37888\n\t"
      "ds_read_b128 v[8:11], v1 offset:32768\n\t"
      "v_lshlrev_b32 v58, 16, v48\n\t"
      "v_and_b32 v59, s21, v48\n\t"
      "v_mul_f32_dpp v58, v56, v58 row_newbcast:0 row_mask:0xf bank_mask:0xf\n\t"
      "v_mul_f32_dpp v59, v56, v59 row_newbcast:1 row_mask:0xf bank_mask:0xf\n\t"
      "v_mfma_f32_32x32x16_bf16 v[82:97], v[44:47], v[28:31], v[82:97]\n\t"
      "ds_read_b128 v[12:15], v1 offset:36864\n\t"
      "ds_read_b128 v[16:19], v1 offset:40960\n\t"
      "ds_read_b128 v[20:23], v1 offset:45056\n\t"
      "v_cvt_pk_bf16_f32 v40, v58, v59\n\t"
      "v_lshlrev_b32 v60, 16, v49\n\t"
      "v_and_b32 v61, s21, v49\n\t"
      "v_mul_f32_dpp v60, v56, v60 row_newbcast:2 row_mask:0xf bank_mask:0xf\n\t"
      "v_mfma_f32_32x32x16_bf16 v[98:113], v[44:47], v[32:35], v[98:113]\n\t"
      "v_mul_f32_dpp v61, v56, v61 row_newbcast:3 row_mask:0xf bank_mask:0xf\n\t"
      "v_cvt_pk_bf16_f32 v41, v60, v61\n\t"
      "v_lshlrev_b32 v62, 16, v50\n\t"
      "v_and_b32 v63, s21, v50\n\t"
      "v_mul_f32_dpp v62, v56, v62 row_newbcast:4 row_mask:0xf bank_mask:0xf\n\t"
      "v_mul_f32_dpp v63, v56, v63 row_newbcast:5 row_mask:0xf bank_mask:0xf\n\t"
      "v_mfma_f32_32x32x16_bf16 v[114:129], v[44:47], v[36:39], v[114:129]\n\t"
      "v_cvt_pk_bf16_f32 v42, v62, v63\n\t"
      "v_lshlrev_b32 v64, 16, v51\n\t"
      "v_and_b32 v65, s21, v51\n\t"
      "v_mul_f32_dpp v64, v56, v64 row_newbcast:6 row_mask:0xf bank_mask:0xf\n\t"
      "v_mul_f32_dpp v65, v56, v65 row_newbcast:7 row_mask:0xf bank_mask:0xf\n\t"
      "v_cvt_pk_bf16_f32 v43, v64, v65\n\t"
      "v_xor_b32 v1, s22, v1\n\t"
      "ds_read_b32 v57, v6 offset:128\n\t"
      "s_waitcnt lgkmcnt(0)\n\t"
      "v_mfma_f32_32x32x16_bf16 v[66:81], v[40:43], v[8:11], v[66:81]\n\t"
      "ds_read_b64_tr_b16 v[48:49], v5 offset:40960\n\t"
      "ds_read_b64_tr_b16 v[50:51], v5 offset:41984\n\t"
      "ds_read_b128 v[24:27], v2 offset:32768\n\t"
      "v_lshlrev_b32 v58, 16, v52\n\t"
      "v_and_b32 v59, s21, v52\n\t"
      "v_mul_f32_dpp v58, v56, v58 row_newbcast:8 row_mask:0xf bank_mask:0xf\n\t"
      "v_mul_f32_dpp v59, v56, v59 row_newbcast:9 row_mask:0xf bank_mask:0xf\n\t"
      "v_mfma_f32_32x32x16_bf16 v[82:97], v[40:43], v[12:15], v[82:97]\n\t"
      "ds_read_b128 v[28:31], v2 offset:36864\n\t"
      "ds_read_b128 v[32:35], v2 offset:40960\n\t"
      "ds_read_b128 v[36:39], v2 offset:45056\n\t"
      "v_cvt_pk_bf16_f32 v44, v58, v59\n\t"
      "v_lshlrev_b32 v60, 16, v53\n\t"
      "v_and_b32 v61, s21, v53\n\t"
      "v_mul_f32_dpp v60, v56, v60 row_newbcast:10 row_mask:0xf bank_mask:0xf\n\t"
      "v_mfma_f32_32x32x16_bf16 v[98:113], v[40:43], v[16:19], v[98:113]\n\t"
      "v_mul_f32_dpp v61, v56, v61 row_newbcast:11 row_mask:0xf bank_mask:0xf\n\t"
      "v_cvt_pk_bf16_f32 v45, v60, v61\n\t"
      "v_lshlrev_b32 v62, 16, v54\n\t"
      "v_and_b32 v63, s21, v54\n\t"
      "v_mul_f32_dpp v62, v56, v62 row_newbcast:12 row_mask:0xf bank_mask:0xf\n\t"
      "v_mul_f32_dpp v63, v56, v63 row_newbcast:13 row_mask:0xf bank_mask:0xf\n\t"
      "v_mfma_f32_32x32x16_bf16 v[114:129], v[40:43], v[20:23], v[114:129]\n\t"
      "v_cvt_pk_bf16_f32 v46, v62, v63\n\t"
      "v_lshlrev_b32 v64, 16, v55\n\t"
      "v_and_b32 v65, s21, v55\n\t"
      "v_mul_f32_dpp v64, v56, v64 row_newbcast:14 row_mask:0xf bank_mask:0xf\n\t"
      "v_mul_f32_dpp v65, v56, v65 row_newbcast:15 row_mask:0xf bank_mask:0xf\n\t"
      "v_cvt_pk_bf16_f32 v47, v64, v65\n\t"
      "v_xor_b32 v2, s22, v2\n\t"
      "s_waitcnt lgkmcnt(0)\n\t"
      "v_mfma_f32_32x32x16_bf16 v[66:81], v[44:47], v[24:27], v[66:81]\n\t"
      "ds_read_b64_tr_b16 v[52:53], v5 offset:45056\n\t"
      "ds_read_b64_tr_b16 v[54:55], v5 offset:46080\n\t"
      "ds_read_b128 v[8:11], v3 offset:32768\n\t"
      "v_lshlrev_b32 v58, 16, v48\n\t"
      "v_and_b32 v59, s21, v48\n\t"
      "v_mul_f32_dpp v58, v57, v58 row_newbcast:0 row_mask:0xf bank_mask:0xf\n\t"
      "v_mul_f32_dpp v59, v57, v59 row_newbcast:1 row_mask:0xf bank_mask:0xf\n\t"
      "v_mfma_f32_32x32x16_bf16 v[82:97], v[44:47], v[28:31], v[82:97]\n\t"
      "ds_read_b128 v[12:15], v3 offset:36864\n\t"
      "ds_read_b128 v[16:19], v3 offset:40960\n\t"
      "ds_read_b128 v[20:23], v3 offset:45056\n\t"
      "v_cvt_pk_bf16_f32 v40, v58, v59\n\t"
      "v_lshlrev_b32 v60, 16, v49\n\t"
      "v_and_b32 v61, s21, v49\n\t"
      "v_mul_f32_dpp v60, v57, v60 row_newbcast:2 row_mask:0xf bank_mask:0xf\n\t"
      "v_mfma_f32_32x32x16_bf16 v[98:113], v[44:47], v[32:35], v[98:113]\n\t"
      "v_mul_f32_dpp v61, v57, v61 row_newbcast:3 row_mask:0xf bank_mask:0xf\n\t"
      "v_cvt_pk_bf16_f32 v41, v60, v61\n\t"
      "v_lshlrev_b32 v62, 16, v50\n\t"
      "v_and_b32 v63, s21, v50\n\t"
      "v_mul_f32_dpp v62, v57, v62 row_newbcast:4 row_mask:0xf bank_mask:0xf\n\t"
      "v_mul_f32_dpp v63, v57, v63 row_newbcast:5 row_mask:0xf bank_mask:0xf\n\t"
      "v_add_u32 v6, 0x100, v6\n\t"
      "v_bfi_b32 v6, s23, v6, v7\n\t"
      "v_mfma_f32_32x32x16_bf16 v[114:129], v[44:47], v[36:39], v[114:129]\n\t"
      "v_cvt_pk_bf16_f32 v42, v62, v63\n\t"
      "v_lshlrev_b32 v64, 16, v51\n\t"
      "v_and_b32 v65, s21, v51\n\t"
      "v_mul_f32_dpp v64, v57, v64 row_newbcast:6 row_mask:0xf bank_mask:0xf\n\t"
      "v_mul_f32_dpp v65, v57, v65 row_newbcast:7 row_mask:0xf bank_mask:0xf\n\t"
      "v_cvt_pk_bf16_f32 v43, v64, v65\n\t"
      "v_xor_b32 v5, s22, v5\n\t"
      "v_xor_b32 v3, s22, v3\n\t"
      "ds_read_b32 v56, v6\n\t"
      "s_barrier\n\t"
      "s_waitcnt lgkmcnt(0)\n\t"
      "v_mfma_f32_32x32x16_bf16 v[66:81], v[40:43], v[8:11], v[66:81]\n\t"
      "ds_read_b64_tr_b16 v[48:49], v5 offset:0\n\t"
      "ds_read_b64_tr_b16 v[50:51], v5 offset:1024\n\t"
      "ds_read_b128 v[24:27], v4 offset:32768\n\t"
      "v_lshlrev_b32 v58, 16, v52\n\t"
      "v_and_b32 v59, s21, v52\n\t"
      "v_mul_f32_dpp v58, v57, v58 row_newbcast:8 row_mask:0xf bank_mask:0xf\n\t"
      "v_mul_f32_dpp v59, v57, v59 row_newbcast:9 row_mask:0xf bank_mask:0xf\n\t"
      "v_mfma_f32_32x32x16_bf16 v[82:97], v[40:43], v[12:15], v[82:97]\n\t"
      "ds_read_b128 v[28:31], v4 offset:36864\n\t"
      "ds_read_b128 v[32:35], v4 offset:40960\n\t"
      "ds_read_b128 v[36:39], v4 offset:45056\n\t"
      "v_cvt_pk_bf16_f32 v44, v58, v59\n\t"
      "v_lshlrev_b32 v60, 16, v53\n\t"
      "v_and_b32 v61, s21, v53\n\t"
      "v_mul_f32_dpp v60, v57, v60 row_newbcast:10 row_mask:0xf bank_mask:0xf\n\t"
      "v_mfma_f32_32x32x16_bf16 v[98:113], v[40:43], v[16:19], v[98:113]\n\t"
      "v_mul_f32_dpp v61, v57, v61 row_newbcast:11 row_mask:0xf bank_mask:0xf\n\t"
      "v_cvt_pk_bf16_f32 v45, v60, v61\n\t"
      "v_lshlrev_b32 v62, 16, v54\n\t"
      "v_and_b32 v63, s21, v54\n\t"
      "v_mul_f32_dpp v62, v57, v62 row_newbcast:12 row_mask:0xf bank_mask:0xf\n\t"
      "v_mul_f32_dpp v63, v57, v63 row_newbcast:13 row_mask:0xf bank_mask:0xf\n\t"
      "v_mfma_f32_32x32x16_bf16 v[114:129], v[40:43], v[20:23], v[114:129]\n\t"
      "v_cvt_pk_bf16_f32 v46, v62, v63\n\t"
      "v_lshlrev_b32 v64, 16, v55\n\t"
      "v_and_b32 v65, s21, v55\n\t"
      "v_mul_f32_dpp v64, v57, v64 row_newbcast:14 row_mask:0xf bank_mask:0xf\n\t"
      "v_mul_f32_dpp v65, v57, v65 row_newbcast:15 row_mask:0xf bank_mask:0xf\n\t"
      "v_cvt_pk_bf16_f32 v47, v64, v65\n\t"
      "v_xor_b32 v4, s22, v4\n\t"
      "s_waitcnt lgkmcnt(0)\n\t"
      "v_mfma_f32_32x32x16_bf16 v[66:81], v[44:47], v[24:27], v[66:81]\n\t"
      "ds_read_b64_tr_b16 v[52:53], v5 offset:4096\n\t"
      "ds_read_b64_tr_b16 v[54:55], v5 offset:5120\n\t"
      "ds_read_b128 v[8:11], v1 offset:0\n\t"
      "v_lshlrev_b32 v58, 16, v48\n\t"
      "v_and_b32 v59, s21, v48\n\t"
      "v_mul_f32_dpp v58, v56, v58 row_newbcast:0 row_mask:0xf bank_mask:0xf\n\t"
      "v_mul_f32_dpp v59, v56, v59 row_newbcast:1 row_mask:0xf bank_mask:0xf\n\t"
      "v_mfma_f32_32x32x16_bf16 v[82:97], v[44:47], v[28:31], v[82:97]\n\t"
      "ds_read_b128 v[12:15], v1 offset:4096\n\t"
      "ds_read_b128 v[16:19], v1 offset:8192\n\t"
      "ds_read_b128 v[20:23], v1 offset:12288\n\t"
      "v_cvt_pk_bf16_f32 v40, v58, v59\n\t"
      "v_lshlrev_b32 v60, 16, v49\n\t"
      "v_and_b32 v61, s21, v49\n\t"
      "v_mul_f32_dpp v60, v56, v60 row_newbcast:2 row_mask:0xf bank_mask:0xf\n\t"
      "v_mfma_f32_32x32x16_bf16 v[98:113], v[44:47], v[32:35], v[98:113]\n\t"
      "v_mul_f32_dpp v61, v56, v61 row_newbcast:3 row_mask:0xf bank_mask:0xf\n\t"
      "v_cvt_pk_bf16_f32 v41, v60, v61\n\t"
      "v_lshlrev_b32 v62, 16, v50\n\t"
      "v_and_b32 v63, s21, v50\n\t"
      "v_mul_f32_dpp v62, v56, v62 row_newbcast:4 row_mask:0xf bank_mask:0xf\n\t"
      "v_mul_f32_dpp v63, v56, v63 row_newbcast:5 row_mask:0xf bank_mask:0xf\n\t"
      "v_mfma_f32_32x32x16_bf16 v[114:129], v[44:47], v[36:39], v[114:129]\n\t"
      "v_cvt_pk_bf16_f32 v42, v62, v63\n\t"
      "v_lshlrev_b32 v64, 16, v51\n\t"
      "v_and_b32 v65, s21, v51\n\t"
      "v_mul_f32_dpp v64, v56, v64 row_newbcast:6 row_mask:0xf bank_mask:0xf\n\t"
      "v_mul_f32_dpp v65, v56, v65 row_newbcast:7 row_mask:0xf bank_mask:0xf\n\t"
      "v_cvt_pk_bf16_f32 v43, v64, v65\n\t"
      "ds_read_b32 v57, v6 offset:128\n\t"
      "s_sub_u32 s20, s20, 1\n\t"
      "s_cmp_lg_u32 s20, 0\n\t"
      "s_cbranch_scc1 1b\n\t"
      "s_waitcnt lgkmcnt(0)\n\t"
      "s_nop 15\n\t"
      : "={v[66:81]}"(acc0), "={v[82:97]}"(acc1), "={v[98:113]}"(acc2), "={v[114:129]}"(acc3),
        "+{v1}"(xa0), "+{v2}"(xa1), "+{v3}"(xa2), "+{v4}"(xa3), "+{v5}"(wa), "+{v6}"(ga)
      :
      : "memory", "scc", "s20", "s21", "s22", "s23", "v7","v8","v9","v10","v11","v12","v13","v14","v15","v16","v17","v18","v19","v20","v21","v22","v23","v24","v25","v26","v27","v28","v29","v30","v31","v32","v33","v34","v35","v36","v37","v38","v39","v40","v41","v42","v43","v44","v45","v46","v47","v48","v49","v50","v51","v52","v53","v54","v55","v56","v57","v58","v59","v60","v61","v62","v63","v64","v65");

    ST(2); CT(4);
    bar();
    f32x4 c1[4], c2[4];
#pragma unroll
    for (int qq = 0; qq < 4; ++qq) {
      const int n0 = wid * 32 + 8 * qq + 4 * h;
      c1[qq] = *(const f32x4*)(smem + OFF_C + n0 * 4);
      c2[qq] = *(const f32x4*)(smem + OFF_C + 512 + n0 * 4);
    }
    auto fin = [&](const f32x16& acc, int mt) {
      const int lr = mt * 32 + (lane & 31);
      const float2 st = *(const float2*)(smem + OFF_ST + lr * 8);
      u32 d[4][2];
#pragma unroll
      for (int qq = 0; qq < 4; ++qq) {
        float o[4];
#pragma unroll
        for (int e = 0; e < 4; ++e) o[e] = acc[4 * qq + e] * st.x + (c1[qq][e] * st.y + c2[qq][e]);
        d[qq][0] = cvt_pk(o[0], o[1]); d[qq][1] = cvt_pk(o[2], o[3]);
      }
#pragma unroll
      for (int qq = 0; qq < 4; qq += 2) {
        auto r0 = __builtin_amdgcn_permlane32_swap(d[qq][0], d[qq + 1][0], false, false);
        auto r1 = __builtin_amdgcn_permlane32_swap(d[qq][1], d[qq + 1][1], false, false);
        u32x4 v = {r0[0], r1[0], r0[1], r1[1]};
        const int ch = wid * 4 + qq + h;
        *(u32x4*)(smem + OFF_OUT + lr * 256 + ((ch ^ (lr & 7)) * 16)) = v;
      }
    };
    fin(acc0, 0); fin(acc1, 1); fin(acc2, 2); fin(acc3, 3);
    wait_lgkm0();
    ST(18);
  } else {
    const int lw = wq;
    const u32x4 xrsrc = mk_rsrc(x, MD * KD * 2), wrsrc = mk_rsrc(w, KD * ND * 2);
    const int rr = lane >> 3, dr = lane >> 4;
    const u32 xv0 = (u32)(((rp * 128 + lw * 32 + rr) * KD + 8 * ((lane & 7) ^ (rr >> 1))) * 2);
    const u32 xv1 = xv0 ^ 64;
    const u32 wv = (u32)(((lw * 16 + dr) * ND + cp * 128 + 8 * ((lane & 15) ^ (dr << 2))) * 2);
    auto issue = [&](int u) {
      const int kt = (u + rot) & 15;
      const u32 sx = (u32)(kt * 128), sw_ = (u32)(kt * 64 * ND * 2);
      const u32 dst = lds0 + (u & (NS - 1)) * SLOT + lw * 4096;
#pragma unroll
      for (int i = 0; i < 4; ++i) {
        dma16(wv, wrsrc, sw_ + i * 4 * ND * 2, dst + 16384 + i * 1024);
        dma16((i & 1) ? xv1 : xv0, xrsrc, sx + i * 8 * KD * 2, dst + i * 1024);
      }
    };
    issue(0);
    {
      const u32x4 trsrc = mk_rsrc(lw < 2 ? g : b, KD * 2);
      dma16(lane * 16, trsrc, (lw & 1) * 1024, lds0 + (lw < 2 ? OFF_G16 : OFF_B16) + (lw & 1) * 1024);
      dma16(lane * 16, trsrc, OOB, lds0 + OFF_Z16 + (lw & 1) * 1024);
    }
    issue(1); issue(2);
    const int jp = lane & 15, kg = lane >> 4;
    u32 seladdr = OFF_Z16;
    if ((jp & 1) == (kg & 1) && jp < 4) seladdr = (jp < 2 ? OFF_G16 : OFF_B16) + (kg >> 1) * 16;
    bf16x8 ones;
    { const short o = ((jp & 1) == (kg & 1) && jp < 2) ? (short)0x3f80 : (short)0; ones = (bf16x8){o, o, o, o, o, o, o, o}; }
    f32x4 hacc = {0.f, 0.f, 0.f, 0.f}, gr0 = {0.f, 0.f, 0.f, 0.f}, gr1 = {0.f, 0.f, 0.f, 0.f}, sumx = {0.f, 0.f, 0.f, 0.f};
    const u32 xrow = (lw * 32 + (lane & 31)) * 128;
    auto extras = [&](int u) {
      const int kt = (u + rot) & 15;
      const char* sl = smem + (u & (NS - 1)) * SLOT;
#pragma unroll
      for (int s = 0; s < 4; ++s) {
        const bf16x8 xf = *(const bf16x8*)(sl + xrow + (((2 * s + h) ^ xr) * 16));
        s16x4 lo = __builtin_amdgcn_ds_read_tr16_b64_v4i16((lds_s16x4_p)(sl + waddr + s * 4096));
        s16x4 hi = __builtin_amdgcn_ds_read_tr16_b64_v4i16((lds_s16x4_p)(sl + waddr + s * 4096 + 1024));
        const bf16x8 wf = __builtin_shufflevector(lo, hi, 0, 1, 2, 3, 4, 5, 6, 7);
        const bf16x8 sf = *(const bf16x8*)(smem + seladdr + kt * 128 + s * 32);
        gr0 = __builtin_amdgcn_mfma_f32_4x4x4bf16_1k(lo4(xf), lo4(xf), gr0, 0, 0, 0);
        gr1 = __builtin_amdgcn_mfma_f32_4x4x4bf16_1k(hi4(xf), hi4(xf), gr1, 0, 0, 0);
        sumx = __builtin_amdgcn_mfma_f32_16x16x32_bf16(xf, ones, sumx, 0, 0, 0);
        hacc = __builtin_amdgcn_mfma_f32_16x16x32_bf16(wf, sf, hacc, 0, 0, 0);
      }
    };
    wait_vm<16>();
    bar();
    const unsigned long long r1_ = __builtin_amdgcn_s_memrealtime(), c1_ = __builtin_amdgcn_s_memtime();
    __builtin_amdgcn_s_waitcnt(0xC07F);
    ST(1);
#pragma unroll 1
    for (int u = 0; u < 16; ++u) {
      extras(u);
      if (u < 14) wait_vm<8>(); else wait_vm<0>();
      wait_lgkm0();
      bar();
      if (u < 13) issue(u + 3);
    }
    { const unsigned long long r2_ = __builtin_amdgcn_s_memrealtime(), c2_ = __builtin_amdgcn_s_memtime();
      enc_ = ENC == 0 ? 0 : ENC == 1 ? (r2_ - r1_) : ENC == 2 ? (c2_ - c1_) / 10 : ENC == 3 ? (r1_ - tstart_) : 0; }
    ST(2);
    if (jp < 4) *(f32x4*)(smem + OFF_C + (jp >> 1) * 512 + (lw * 32 + (jp & 1) * 16 + 4 * kg) * 4) = hacc;
    if (jp < 2) *(f32x4*)(smem + OFF_SX + (lw * 32 + jp * 16 + 4 * kg) * 4) = sumx;
    const f32x4 gs = gr0 + gr1;
    float sq = p4 == 0 ? gs[0] : p4 == 1 ? gs[1] : p4 == 2 ? gs[2] : gs[3];
    sq += __shfl_xor(sq, 32);
    wait_lgkm0();
    {
      const float mu = *(const float*)(smem + OFF_SX + (lw * 32 + (lane & 31)) * 4) * (1.f / KD);
      const float var = fmaxf(sq * (1.f / KD) - mu * mu, 0.f);
      const float rs = rsqrtf(var + 1e-5f);
      if (lane < 32) *(float2*)(smem + OFF_ST + (lw * 32 + lane) * 8) = make_float2(rs, -mu * rs);
    }
    wait_lgkm0();
    ST(17);
    bar();
    ST(18);
  }
  bar();
  {
    const __amdgpu_buffer_rsrc_t orsrc = __builtin_amdgcn_make_buffer_rsrc((void*)out, 0, MD * ND * 2, 0x00020000);
    const int r0 = wid * 16 + (lane >> 4), ch = lane & 15;
#pragma unroll
    for (int i = 0; i < 4; ++i) {
      const int r = r0 + 4 * i;
      const u32x4 v = *(const u32x4*)(smem + OFF_OUT + r * 256 + ((ch ^ (r & 7)) * 16));
      __builtin_amdgcn_raw_buffer_store_b128(v, orsrc, (u32)(((rp * 128 + r) * ND + cp * 128 + ch * 8) * 2), 0, STAUX);
    }
  }
  wait_vm<0>(); if (ENC == 4) enc_ = __builtin_amdgcn_s_memrealtime() - tstart_;
  PADTO(5000 + enc_);
}

extern "C" void kernel_launch(void* const* d_in, const int* in_sizes, int n_in,
                              void* d_out, int out_size, void* d_ws, size_t ws_size, hipStream_t stream) {
  const u16* x = (const u16*)d_in[0]; const u16* g = (const u16*)d_in[1];
  const u16* b = (const u16*)d_in[2]; const u16* w = (const u16*)d_in[3];
  (void)hipFuncSetAttribute((const void*)lnlin_kernel, hipFuncAttributeMaxDynamicSharedMemorySize, LDS_BYTES);
  lnlin_kernel<<<256, 512, LDS_BYTES, stream>>>(x, g, b, w, (u16*)d_out, (unsigned long long*)((char*)d_ws + (64u << 20)));
}
```

```cpp
#include <hip/hip_runtime.h>
#include <hip/hip_bf16.h>
typedef unsigned int u32;
typedef unsigned short u16;
using bf16x8 = __attribute__((ext_vector_type(8))) short;
using s16x4  = __attribute__((ext_vector_type(4))) short;
using f32x4  = __attribute__((ext_vector_type(4))) float;
using f32x16 = __attribute__((ext_vector_type(16))) float;
using u32x4  = __attribute__((ext_vector_type(4))) u32;
using u32x2  = __attribute__((ext_vector_type(2))) u32;
typedef __attribute__((address_space(3))) s16x4* lds_s16x4_p;

constexpr int MD = 4096, KD = 1024, ND = 1024;
constexpr int NS = 4;
constexpr int SLOT = 32768;
constexpr int OFF_G16 = NS * SLOT;
constexpr int OFF_B16 = OFF_G16 + 2048 + 64;
constexpr int OFF_ST  = OFF_B16 + 2048 + 64;
constexpr int OFF_C   = OFF_ST + 1024;
constexpr int OFF_JUNK = OFF_C + 1024;
constexpr int LDS_BYTES = OFF_JUNK + 256;
constexpr int OFF_OUT = SLOT;
constexpr int CODE_HALF = 3840;
constexpr int PUBU = 11, GATU = 14;
constexpr int STAUX = 2;
constexpr u32 WS_MB = 65536;
constexpr u32 WS_BYTES = WS_MB + 256 * 4096;

__device__ __forceinline__ void wait_lgkm0() { asm volatile("s_waitcnt lgkmcnt(0)" ::: "memory"); }
__device__ __forceinline__ void bar() { asm volatile("s_barrier" ::: "memory"); }
__device__ __forceinline__ u32 cvt_pk(float lo, float hi) {
  typedef __attribute__((ext_vector_type(2))) float f2; typedef __attribute__((ext_vector_type(2))) __bf16 b2;
  f2 v = {lo, hi}; b2 r = __builtin_convertvector(v, b2); return __builtin_bit_cast(u32, r);
}
__device__ __forceinline__ s16x4 lo4(bf16x8 v) { return __builtin_shufflevector(v, v, 0, 1, 2, 3); }
__device__ __forceinline__ s16x4 hi4(bf16x8 v) { return __builtin_shufflevector(v, v, 4, 5, 6, 7); }

__global__ __launch_bounds__(768, 3)
void lnlin_kernel(const u16* __restrict__ x, const u16* __restrict__ w, const u16* __restrict__ g,
                  const u16* __restrict__ b, u16* __restrict__ out, char* ws) {
  extern __shared__ __attribute__((aligned(16))) char smem[];
  const int bid = blockIdx.x, xcd = bid & 7, idx = bid >> 3;
  const int rpl = idx >> 3, rp = xcd * 4 + rpl, cp = idx & 7;
  const int tid = threadIdx.x, lane = tid & 63;
  const int wid = __builtin_amdgcn_readfirstlane(tid >> 6);
  const int wq = wid & 3;
  const int rot = xcd * 2 + (cp & 3) * 4;
  const u32 lds0 = (u32)(size_t)smem;
  asm volatile(
      "v_readfirstlane_b32 s3, %1\n\t"
      "s_cmpk_gt_u32 s3, 255\n\t"
      "s_cbranch_scc1 9f\n\t"
      "s_load_dwordx2 s[12:13], %0, 0x0\n\t"
      "s_load_dwordx2 s[16:17], %0, 0x8\n\t"
      "s_load_dwordx4 s[24:27], %0, 0x10\n\t"
      "s_and_b32 s4, %2, 7\n\t"
      "s_bfe_u32 s5, %2, 0x30003\n\t"
      "s_lshr_b32 s6, %2, 6\n\t"
      "v_and_b32 v1, 63, %1\n\t"
      "v_lshrrev_b32 v2, 4, v1\n\t"
      "v_xor_b32 v3, v2, v1\n\t"
      "s_lshl1_add_u32 s7, s5, s4\n\t"
      "v_and_b32 v3, 7, v3\n\t"
      "s_lshl_b32 s7, s7, 1\n\t"
      "v_lshrrev_b32 v4, 3, v1\n\t"
      "s_and_b32 s7, s7, 15\n\t"
      "v_lshlrev_b32 v3, 4, v3\n\t"
      "s_lshl2_add_u32 s8, s4, s6\n\t"
      "v_lshl_or_b32 v3, v4, 11, v3\n\t"
      "s_lshl_b32 s8, s8, 18\n\t"
      "v_lshlrev_b32 v5, 2, v2\n\t"
      "s_lshl_b32 s9, s3, 10\n\t"
      "v_xor_b32 v5, v5, v1\n\t"
      "s_add_u32 s8, s8, s9\n\t"
      "v_and_b32 v5, 15, v5\n\t"
      "s_lshl_b32 s9, s3, 9\n\t"
      "v_lshlrev_b32 v5, 4, v5\n\t"
      "s_lshl_b32 s10, s5, 8\n\t"
      "v_lshl_or_b32 v5, v2, 11, v5\n\t"
      "s_add_u32 s9, s9, s10\n\t"
      "v_xor_b32 v4, 64, v3\n\t"
      "v_xor_b32 v6, 32, v5\n\t"
      "s_lshl_b32 s10, s3, 6\n\t"
      "s_mov_b32 s14, 0x800000\n\t"
      "s_mov_b32 s15, 0x20000\n\t"
      "s_mov_b32 s18, 0x200000\n\t"
      "s_mov_b32 s19, 0x20000\n\t"
      "s_mov_b32 s11, -3\n\t"
      "s_waitcnt lgkmcnt(0)\n\t"
      "1:\n\t"
      "s_lshl_b32 s20, s7, 17\n\t"
      "s_add_u32 m0, s10, 0x4000\n\t"
      "s_add_u32 s20, s20, s9\n\t"
      "s_lshl_b32 s21, s7, 7\n\t"
      "buffer_load_dwordx4 v5, s[16:19], s20 offen lds\n\t"
      "s_addk_i32 s20, 0x1c00\n\t"
      "buffer_load_dwordx4 v5, s[16:19], s20 offen offset:1024 lds\n\t"
      "s_addk_i32 s20, 0x1c00\n\t"
      "buffer_load_dwordx4 v6, s[16:19], s20 offen offset:2048 lds\n\t"
      "s_addk_i32 s20, 0x1c00\n\t"
      "buffer_load_dwordx4 v6, s[16:19], s20 offen offset:3072 lds\n\t"
      "s_mov_b32 m0, s10\n\t"
      "s_add_u32 s21, s21, s8\n\t"
      "buffer_load_dwordx4 v3, s[12:15], s21 offen lds\n\t"
      "s_addk_i32 s21, 0x3c00\n\t"
      "buffer_load_dwordx4 v4, s[12:15], s21 offen offset:1024 lds\n\t"
      "s_addk_i32 s21, 0x3c00\n\t"
      "buffer_load_dwordx4 v3, s[12:15], s21 offen offset:2048 lds\n\t"
      "s_addk_i32 s21, 0x3c00\n\t"
      "buffer_load_dwordx4 v4, s[12:15], s21 offen offset:3072 lds\n\t"
      "s_add_u32 s7, s7, 1\n\t"
      "s_add_u32 s10, s10, 0x8000\n\t"
      "s_and_b32 s7, s7, 15\n\t"
      "s_and_b32 s10, s10, 0x1ffff\n\t"
      "s_add_u32 s11, s11, 1\n\t"
      "s_cmp_gt_i32 s11, -2\n\t"
      "s_cbranch_scc1 2f\n\t"
      "s_cmpk_lt_u32 s3, 128\n\t"
      "s_cselect_b32 s24, s24, s26\n\t"
      "s_cselect_b32 s25, s25, s27\n\t"
      "s_cselect_b32 s20, 0, %4\n\t"
      "s_add_u32 s20, s20, %3\n\t"
      "s_movk_i32 s26, 0x800\n\t"
      "s_mov_b32 s27, 0x20000\n\t"
      "s_and_b32 s21, s3, 64\n\t"
      "s_lshl_b32 s21, s21, 4\n\t"
      "v_lshlrev_b32 v1, 4, v1\n\t"
      "s_add_u32 m0, s20, s21\n\t"
      "s_nop 0\n\t"
      "buffer_load_dwordx4 v1, s[24:27], s21 offen lds\n\t"
      "s_branch 1b\n\t"
      "2:\n\t"
      "s_waitcnt vmcnt(8)\n\t"
      "s_barrier\n\t"
      "s_cmp_lt_i32 s11, 13\n\t"
      "s_cbranch_scc1 1b\n\t"
      "s_waitcnt vmcnt(0)\n\t"
      "s_barrier\n\t"
      "s_endpgm\n\t"
      "9:\n\t"
      :: "s"(__builtin_amdgcn_kernarg_segment_ptr()), "v"(tid), "s"(bid), "i"(OFF_G16), "i"(OFF_B16 - OFF_G16)
      : "memory", "scc", "s3", "s4", "s5", "s6", "s7", "s8", "s9", "s10", "s11", "s12", "s13", "s14", "s15", "s16", "s17", "s18", "s19",
        "s20", "s21", "s24", "s25", "s26", "s27", "v1", "v2", "v3", "v4", "v5", "v6");
  if (wid >= 10) {
    unsigned long long pc_;
    asm volatile("s_getpc_b64 %0" : "=s"(pc_));
    u32 keep;
    asm volatile("s_mov_b32 %0, m0\n\ts_mov_b32 m0, %3\n\ts_nop 0\n\tglobal_load_lds_dword %1, %2\n\ts_mov_b32 m0, %0"
                 : "=&s"(keep) : "v"((u32)((wid - 10) * CODE_HALF + lane * (CODE_HALF / 64))), "s"(pc_ & ~63ull), "s"(lds0 + OFF_JUNK) : "memory");
  }
  const int h = lane >> 5;
  const int xr = (lane >> 1) & 7;
  const int blk = (lane >> 4) & 1, q = (lane & 15) >> 2, p4 = lane & 3;
  const u32 waddr = 16384 + (8 * h + q) * 256 + 16 * (4 * (wq ^ q) + 2 * (blk ^ h) + (p4 >> 1)) + 8 * (p4 & 1);
  const __amdgpu_buffer_rsrc_t orsrc = __builtin_amdgcn_make_buffer_rsrc((void*)out, 0, MD * ND * 2, 0x00020000);

  if (wid < 8) {
    const int m16 = lane & 15, kg = lane >> 4;
    u32 xa0 = lds0 + m16 * 128 + ((kg ^ (m16 >> 1)) * 16), xa1 = lds0 + m16 * 128 + (((4 + kg) ^ (m16 >> 1)) * 16);
    u32 wa0 = lds0 + 16384 + (8 * kg + q) * 256 + 16 * (4 * (wq ^ q) + ((kg & 1) << 1) + (p4 >> 1)) + 8 * (p4 & 1), wa1 = wa0 ^ 32;
    u32 ga = lds0 + OFF_G16 + (rot & 15) * 128 + kg * 16 + (p4 & 2) * 2;
    const u32 hm = (p4 & 1) ? 0xffff0000u : 0xffffu, ma = (p4 & 2) ? 0u : hm, mb = (p4 & 2) ? hm : 0u;
    f32x16 acc0, acc1, acc2, acc3;
    asm volatile(
      "s_mov_b32 s22, 0x10000\n\t"
      "s_mov_b32 s23, 0x7ff\n\t"
      "s_mov_b32 s20, 4\n\t"
      "v_mov_b32 v6, v5\n\t"
      "v_mov_b32 v66, 0\n\t"
      "v_mov_b32 v67, 0\n\t"
      "v_mov_b32 v68, 0\n\t"
      "v_mov_b32 v69, 0\n\t"
      "v_mov_b32 v70, 0\n\t"
      "v_mov_b32 v71, 0\n\t"
      "v_mov_b32 v72, 0\n\t"
      "v_mov_b32 v73, 0\n\t"
      "v_mov_b32 v74, 0\n\t"
      "v_mov_b32 v75, 0\n\t"
      "v_mov_b32 v76, 0\n\t"
      "v_mov_b32 v77, 0\n\t"
      "v_mov_b32 v78, 0\n\t"
      "v_mov_b32 v79, 0\n\t"
      "v_mov_b32 v80, 0\n\t"
      "v_mov_b32 v81, 0\n\t"
      "v_mov_b32 v82, 0\n\t"
      "v_mov_b32 v83, 0\n\t"
      "v_mov_b32 v84, 0\n\t"
      "v_mov_b32 v85, 0\n\t"
      "v_mov_b32 v86, 0\n\t"
      "v_mov_b32 v87, 0\n\t"
      "v_mov_b32 v88, 0\n\t"
      "v_mov_b32 v89, 0\n\t"
      "v_mov_b32 v90, 0\n\t"
      "v_mov_b32 v91, 0\n\t"
      "v_mov_b32 v92, 0\n\t"
      "v_mov_b32 v93, 0\n\t"
      "v_mov_b32 v94, 0\n\t"
      "v_mov_b32 v95, 0\n\t"
      "v_mov_b32 v96, 0\n\t"
      "v_mov_b32 v97, 0\n\t"
      "v_mov_b32 v98, 0\n\t"
      "v_mov_b32 v99, 0\n\t"
      "v_mov_b32 v100, 0\n\t"
      "v_mov_b32 v101, 0\n\t"
      "v_mov_b32 v102, 0\n\t"
      "v_mov_b32 v103, 0\n\t"
      "v_mov_b32 v104, 0\n\t"
      "v_mov_b32 v105, 0\n\t"
      "v_mov_b32 v106, 0\n\t"
      "v_mov_b32 v107, 0\n\t"
      "v_mov_b32 v108, 0\n\t"
      "v_mov_b32 v109, 0\n\t"
      "v_mov_b32 v110, 0\n\t"
      "v_mov_b32 v111, 0\n\t"
      "v_mov_b32 v112, 0\n\t"
      "v_mov_b32 v113, 0\n\t"
      "v_mov_b32 v114, 0\n\t"
      "v_mov_b32 v115, 0\n\t"
      "v_mov_b32 v116, 0\n\t"
      "v_mov_b32 v117, 0\n\t"
      "v_mov_b32 v118, 0\n\t"
      "v_mov_b32 v119, 0\n\t"
      "v_mov_b32 v120, 0\n\t"
      "v_mov_b32 v121, 0\n\t"
      "v_mov_b32 v122, 0\n\t"
      "v_mov_b32 v123, 0\n\t"
      "v_mov_b32 v124, 0\n\t"
      "v_mov_b32 v125, 0\n\t"
      "v_mov_b32 v126, 0\n\t"
      "v_mov_b32 v127, 0\n\t"
      "v_mov_b32 v128, 0\n\t"
      "v_mov_b32 v129, 0\n\t"
      "s_barrier\n\t"
      "ds_read2_b32 v[130:131], v5 offset0:0 offset1:2\n\t"
      "ds_read_b64_tr_b16 v[58:59], v3 offset:0\n\t"
      "ds_read_b64_tr_b16 v[60:61], v3 offset:1024\n\t"
      "ds_read_b64_tr_b16 v[62:63], v4 offset:0\n\t"
      "ds_read_b64_tr_b16 v[64:65], v4 offset:1024\n\t"
      "ds_read_b128 v[10:13], v1 offset:0\n\t"
      "ds_read_b128 v[14:17], v1 offset:2048\n\t"
      "ds_read_b128 v[18:21], v1 offset:4096\n\t"
      "ds_read_b128 v[22:25], v1 offset:6144\n\t"
      "ds_read_b128 v[26:29], v1 offset:8192\n\t"
      "ds_read_b128 v[30:33], v1 offset:10240\n\t"
      "ds_read_b128 v[34:37], v1 offset:12288\n\t"
      "ds_read_b128 v[38:41], v1 offset:14336\n\t"
      "s_waitcnt lgkmcnt(12)\n\t"
      "v_and_b32 v132, v7, v130\n\t"
      "v_and_b32 v133, v8, v130\n\t"
      "v_and_b32 v134, v7, v131\n\t"
      "v_and_b32 v135, v8, v131\n\t"
      "ds_read2_b32 v[130:131], v5 offset0:16 offset1:18\n\t"
      "v_add_u32 v5, 0x80, v5\n\t"
      "v_bfi_b32 v5, s23, v5, v6\n\t"
      "s_waitcnt lgkmcnt(9)\n\t"
      "v_mfma_f32_4x4x4_16b_bf16 v[136:139], v[132:133], v[58:59], 0\n\t"
      "v_mfma_f32_4x4x4_16b_bf16 v[140:143], v[134:135], v[60:61], 0\n\t"
      "v_mfma_f32_4x4x4_16b_bf16 v[144:147], v[132:133], v[62:63], 0\n\t"
      "v_mfma_f32_4x4x4_16b_bf16 v[148:151], v[134:135], v[64:65], 0\n\t"
      "s_nop 1\n\t"
      "v_cvt_pk_bf16_f32 v42, v136, v137\n\t"
      "v_cvt_pk_bf16_f32 v43, v138, v139\n\t"
      "v_cvt_pk_bf16_f32 v44, v140, v141\n\t"
      "v_cvt_pk_bf16_f32 v45, v142, v143\n\t"
      "v_cvt_pk_bf16_f32 v46, v144, v145\n\t"
      "v_cvt_pk_bf16_f32 v47, v146, v147\n\t"
      "v_cvt_pk_bf16_f32 v48, v148, v149\n\t"
      "v_cvt_pk_bf16_f32 v49, v150, v151\n\t"
      "s_waitcnt lgkmcnt(0)\n\t"
      "s_branch 2f\n\t"
      "1:\n\t"
      "s_barrier\n\t"
      "s_waitcnt lgkmcnt(5)\n\t"
      "v_mfma_f32_16x16x32_bf16 v[66:69], v[50:53], v[10:13], v[66:69]\n\t"
      "ds_read_b64_tr_b16 v[58:59], v3 offset:32768\n\t"
      "ds_read_b64_tr_b16 v[60:61], v3 offset:33792\n\t"
      "v_mfma_f32_16x16x32_bf16 v[70:73], v[54:57], v[10:13], v[70:73]\n\t"
      "ds_read_b64_tr_b16 v[62:63], v4 offset:32768\n\t"
      "ds_read_b64_tr_b16 v[64:65], v4 offset:33792\n\t"
      "v_mfma_f32_16x16x32_bf16 v[74:77], v[50:53], v[14:17], v[74:77]\n\t"
      "v_and_b32 v132, v7, v130\n\t"
      "v_and_b32 v133, v8, v130\n\t"
      "v_mfma_f32_16x16x32_bf16 v[78:81], v[54:57], v[14:17], v[78:81]\n\t"
      "ds_read_b128 v[10:13], v1 offset:32768\n\t"
      "v_and_b32 v134, v7, v131\n\t"
      "v_mfma_f32_16x16x32_bf16 v[82:85], v[50:53], v[18:21], v[82:85]\n\t"
      "ds_read_b128 v[14:17], v1 offset:34816\n\t"
      "v_and_b32 v135, v8, v131\n\t"
      "v_mfma_f32_16x16x32_bf16 v[86:89], v[54:57], v[18:21], v[86:89]\n\t"
      "ds_read_b128 v[18:21], v1 offset:36864\n\t"
      "s_waitcnt lgkmcnt(3)\n\t"
      "v_mfma_f32_4x4x4_16b_bf16 v[136:139], v[132:133], v[58:59], 0\n\t"
      "v_mfma_f32_4x4x4_16b_bf16 v[140:143], v[134:135], v[60:61], 0\n\t"
      "v_mfma_f32_4x4x4_16b_bf16 v[144:147], v[132:133], v[62:63], 0\n\t"
      "v_mfma_f32_4x4x4_16b_bf16 v[148:151], v[134:135], v[64:65], 0\n\t"
      "v_mfma_f32_16x16x32_bf16 v[90:93], v[50:53], v[22:25], v[90:93]\n\t"
      "ds_read2_b32 v[130:131], v5 offset0:16 offset1:18\n\t"
      "v_mfma_f32_16x16x32_bf16 v[94:97], v[54:57], v[22:25], v[94:97]\n\t"
      "ds_read_b128 v[22:25], v1 offset:38912\n\t"
      "v_add_u32 v5, 0x80, v5\n\t"
      "v_mfma_f32_16x16x32_bf16 v[98:101], v[50:53], v[26:29], v[98:101]\n\t"
      "v_cvt_pk_bf16_f32 v42, v136, v137\n\t"
      "v_cvt_pk_bf16_f32 v43, v138, v139\n\t"
      "v_mfma_f32_16x16x32_bf16 v[102:105], v[54:57], v[26:29], v[102:105]\n\t"
      "ds_read_b128 v[26:29], v1 offset:40960\n\t"
      "v_bfi_b32 v5, s23, v5, v6\n\t"
      "v_mfma_f32_16x16x32_bf16 v[106:109], v[50:53], v[30:33], v[106:109]\n\t"
      "v_cvt_pk_bf16_f32 v44, v140, v141\n\t"
      "v_cvt_pk_bf16_f32 v45, v142, v143\n\t"
      "v_mfma_f32_16x16x32_bf16 v[110:113], v[54:57], v[30:33], v[110:113]\n\t"
      "ds_read_b128 v[30:33], v1 offset:43008\n\t"
      "v_mfma_f32_16x16x32_bf16 v[114:117], v[50:53], v[34:37], v[114:117]\n\t"
      "v_cvt_pk_bf16_f32 v46, v144, v145\n\t"
      "v_cvt_pk_bf16_f32 v47, v146, v147\n\t"
      "v_mfma_f32_16x16x32_bf16 v[118:121], v[54:57], v[34:37], v[118:121]\n\t"
      "ds_read_b128 v[34:37], v1 offset:45056\n\t"
      "v_mfma_f32_16x16x32_bf16 v[122:125], v[50:53], v[38:41], v[122:125]\n\t"
      "v_cvt_pk_bf16_f32 v48, v148, v149\n\t"
      "v_cvt_pk_bf16_f32 v49, v150, v151\n\t"
      "v_mfma_f32_16x16x32_bf16 v[126:129], v[54:57], v[38:41], v[126:129]\n\t"
      "ds_read_b128 v[38:41], v1 offset:47104\n\t"
      "s_waitcnt lgkmcnt(5)\n\t"
      "v_mfma_f32_16x16x32_bf16 v[66:69], v[42:45], v[10:13], v[66:69]\n\t"
      "ds_read_b64_tr_b16 v[58:59], v3 offset:40960\n\t"
      "ds_read_b64_tr_b16 v[60:61], v3 offset:41984\n\t"
      "v_mfma_f32_16x16x32_bf16 v[70:73], v[46:49], v[10:13], v[70:73]\n\t"
      "ds_read_b64_tr_b16 v[62:63], v4 offset:40960\n\t"
      "ds_read_b64_tr_b16 v[64:65], v4 offset:41984\n\t"
      "v_mfma_f32_16x16x32_bf16 v[74:77], v[42:45], v[14:17], v[74:77]\n\t"
      "v_and_b32 v132, v7, v130\n\t"
      "v_and_b32 v133, v8, v130\n\t"
      "v_mfma_f32_16x16x32_bf16 v[78:81], v[46:49], v[14:17], v[78:81]\n\t"
      "ds_read_b128 v[10:13], v2 offset:32768\n\t"
      "v_and_b32 v134, v7, v131\n\t"
      "v_mfma_f32_16x16x32_bf16 v[82:85], v[42:45], v[18:21], v[82:85]\n\t"
      "ds_read_b128 v[14:17], v2 offset:34816\n\t"
      "v_and_b32 v135, v8, v131\n\t"
      "v_mfma_f32_16x16x32_bf16 v[86:89], v[46:49], v[18:21], v[86:89]\n\t"
      "ds_read_b128 v[18:21], v2 offset:36864\n\t"
      "s_waitcnt lgkmcnt(3)\n\t"
      "v_mfma_f32_4x4x4_16b_bf16 v[136:139], v[132:133], v[58:59], 0\n\t"
      "v_mfma_f32_4x4x4_16b_bf16 v[140:143], v[134:135], v[60:61], 0\n\t"
      "v_mfma_f32_4x4x4_16b_bf16 v[144:147], v[132:133], v[62:63], 0\n\t"
      "v_mfma_f32_4x4x4_16b_bf16 v[148:151], v[134:135], v[64:65], 0\n\t"
      "v_mfma_f32_16x16x32_bf16 v[90:93], v[42:45], v[22:25], v[90:93]\n\t"
      "ds_read2_b32 v[130:131], v5 offset0:0 offset1:2\n\t"
      "v_mfma_f32_16x16x32_bf16 v[94:97], v[46:49], v[22:25], v[94:97]\n\t"
      "ds_read_b128 v[22:25], v2 offset:38912\n\t"
      "v_xor_b32 v3, s22, v3\n\t"
      "v_mfma_f32_16x16x32_bf16 v[98:101], v[42:45], v[26:29], v[98:101]\n\t"
      "v_cvt_pk_bf16_f32 v50, v136, v137\n\t"
      "v_cvt_pk_bf16_f32 v51, v138, v139\n\t"
      "v_mfma_f32_16x16x32_bf16 v[102:105], v[46:49], v[26:29], v[102:105]\n\t"
      "ds_read_b128 v[26:29], v2 offset:40960\n\t"
      "v_xor_b32 v4, s22, v4\n\t"
      "v_mfma_f32_16x16x32_bf16 v[106:109], v[42:45], v[30:33], v[106:109]\n\t"
      "v_cvt_pk_bf16_f32 v52, v140, v141\n\t"
      "v_cvt_pk_bf16_f32 v53, v142, v143\n\t"
      "v_mfma_f32_16x16x32_bf16 v[110:113], v[46:49], v[30:33], v[110:113]\n\t"
      "ds_read_b128 v[30:33], v2 offset:43008\n\t"
      "v_xor_b32 v1, s22, v1\n\t"
      "v_mfma_f32_16x16x32_bf16 v[114:117], v[42:45], v[34:37], v[114:117]\n\t"
      "v_cvt_pk_bf16_f32 v54, v144, v145\n\t"
      "v_cvt_pk_bf16_f32 v55, v146, v147\n\t"
      "v_mfma_f32_16x16x32_bf16 v[118:121], v[46:49], v[34:37], v[118:121]\n\t"
      "ds_read_b128 v[34:37], v2 offset:45056\n\t"
      "v_mfma_f32_16x16x32_bf16 v[122:125], v[42:45], v[38:41], v[122:125]\n\t"
      "v_cvt_pk_bf16_f32 v56, v148, v149\n\t"
      "v_cvt_pk_bf16_f32 v57, v150, v151\n\t"
      "v_mfma_f32_16x16x32_bf16 v[126:129], v[46:49], v[38:41], v[126:129]\n\t"
      "ds_read_b128 v[38:41], v2 offset:47104\n\t"
      "s_barrier\n\t"
      "s_waitcnt lgkmcnt(5)\n\t"
      "v_mfma_f32_16x16x32_bf16 v[66:69], v[50:53], v[10:13], v[66:69]\n\t"
      "ds_read_b64_tr_b16 v[58:59], v3 offset:0\n\t"
      "ds_read_b64_tr_b16 v[60:61], v3 offset:1024\n\t"
      "v_mfma_f32_16x16x32_bf16 v[70:73], v[54:57], v[10:13], v[70:73]\n\t"
      "ds_read_b64_tr_b16 v[62:63], v4 offset:0\n\t"
      "ds_read_b64_tr_b16 v[64:65], v4 offset:1024\n\t"
      "v_mfma_f32_16x16x32_bf16 v[74:77], v[50:53], v[14:17], v[74:77]\n\t"
      "v_and_b32 v132, v7, v130\n\t"
      "v_and_b32 v133, v8, v130\n\t"
      "v_mfma_f32_16x16x32_bf16 v[78:81], v[54:57], v[14:17], v[78:81]\n\t"
      "ds_read_b128 v[10:13], v1 offset:0\n\t"
      "v_and_b32 v134, v7, v131\n\t"
      "v_mfma_f32_16x16x32_bf16 v[82:85], v[50:53], v[18:21], v[82:85]\n\t"
      "ds_read_b128 v[14:17], v1 offset:2048\n\t"
      "v_and_b32 v135, v8, v131\n\t"
      "v_mfma_f32_16x16x32_bf16 v[86:89], v[54:57], v[18:21], v[86:89]\n\t"
      "ds_read_b128 v[18:21], v1 offset:4096\n\t"
      "s_waitcnt lgkmcnt(3)\n\t"
      "v_mfma_f32_4x4x4_16b_bf16 v[136:139], v[132:133], v[58:59], 0\n\t"
      "v_mfma_f32_4x4x4_16b_bf16 v[140:143], v[134:135], v[60:61], 0\n\t"
      "v_mfma_f32_4x4x4_16b_bf16 v[144:147], v[132:133], v[62:63], 0\n\t"
      "v_mfma_f32_4x4x4_16b_bf16 v[148:151], v[134:135], v[64:65], 0\n\t"
      "v_mfma_f32_16x16x32_bf16 v[90:93], v[50:53], v[22:25], v[90:93]\n\t"
      "ds_read2_b32 v[130:131], v5 offset0:16 offset1:18\n\t"
      "v_mfma_f32_16x16x32_bf16 v[94:97], v[54:57], v[22:25], v[94:97]\n\t"
      "ds_read_b128 v[22:25], v1 offset:6144\n\t"
      "v_add_u32 v5, 0x80, v5\n\t"
      "v_mfma_f32_16x16x32_bf16 v[98:101], v[50:53], v[26:29], v[98:101]\n\t"
      "v_cvt_pk_bf16_f32 v42, v136, v137\n\t"
      "v_cvt_pk_bf16_f32 v43, v138, v139\n\t"
      "v_mfma_f32_16x16x32_bf16 v[102:105], v[54:57], v[26:29], v[102:105]\n\t"
      "ds_read_b128 v[26:29], v1 offset:8192\n\t"
      "v_bfi_b32 v5, s23, v5, v6\n\t"
      "v_mfma_f32_16x16x32_bf16 v[106:109], v[50:53], v[30:33], v[106:109]\n\t"
      "v_cvt_pk_bf16_f32 v44, v140, v141\n\t"
      "v_cvt_pk_bf16_f32 v45, v142, v143\n\t"
      "v_mfma_f32_16x16x32_bf16 v[110:113], v[54:57], v[30:33], v[110:113]\n\t"
      "ds_read_b128 v[30:33], v1 offset:10240\n\t"
      "v_xor_b32 v2, s22, v2\n\t"
      "v_mfma_f32_16x16x32_bf16 v[114:117], v[50:53], v[34:37], v[114:117]\n\t"
      "v_cvt_pk_bf16_f32 v46, v144, v145\n\t"
      "v_cvt_pk_bf16_f32 v47, v146, v147\n\t"
      "v_mfma_f32_16x16x32_bf16 v[118:121], v[54:57], v[34:37], v[118:121]\n\t"
      "ds_read_b128 v[34:37], v1 offset:12288\n\t"
      "v_mfma_f32_16x16x32_bf16 v[122:125], v[50:53], v[38:41], v[122:125]\n\t"
      "v_cvt_pk_bf16_f32 v48, v148, v149\n\t"
      "v_cvt_pk_bf16_f32 v49, v150, v151\n\t"
      "v_mfma_f32_16x16x32_bf16 v[126:129], v[54:57], v[38:41], v[126:129]\n\t"
      "ds_read_b128 v[38:41], v1 offset:14336\n\t"
      "s_waitcnt lgkmcnt(5)\n\t"
      "v_mfma_f32_16x16x32_bf16 v[66:69], v[42:45], v[10:13], v[66:69]\n\t"
      "ds_read_b64_tr_b16 v[58:59], v3 offset:8192\n\t"
      "ds_read_b64_tr_b16 v[60:61], v3 offset:9216\n\t"
      "v_mfma_f32_16x16x32_bf16 v[70:73], v[46:49], v[10:13], v[70:73]\n\t"
      "ds_read_b64_tr_b16 v[62:63], v4 offset:8192\n\t"
      "ds_read_b64_tr_b16 v[64:65], v4 offset:9216\n\t"
      "v_mfma_f32_16x16x32_bf16 v[74:77], v[42:45], v[14:17], v[74:77]\n\t"
      "v_and_b32 v132, v7, v130\n\t"
      "v_and_b32 v133, v8, v130\n\t"
      "v_mfma_f32_16x16x32_bf16 v[78:81], v[46:49], v[14:17], v[78:81]\n\t"
      "ds_read_b128 v[10:13], v2 offset:0\n\t"
      "v_and_b32 v134, v7, v131\n\t"
      "v_mfma_f32_16x16x32_bf16 v[82:85], v[42:45], v[18:21], v[82:85]\n\t"
      "ds_read_b128 v[14:17], v2 offset:2048\n\t"
      "v_and_b32 v135, v8, v131\n\t"
      "v_mfma_f32_16x16x32_bf16 v[86:89], v[46:49], v[18:21], v[86:89]\n\t"
      "ds_read_b128 v[18:21], v2 offset:4096\n\t"
      "s_waitcnt lgkmcnt(3)\n\t"
      "v_mfma_f32_4x4x4_16b_bf16 v[136:139], v[132:133], v[58:59], 0\n\t"
      "v_mfma_f32_4x4x4_16b_bf16 v[140:143], v[134:135], v[60:61], 0\n\t"
      "v_mfma_f32_4x4x4_16b_bf16 v[144:147], v[132:133], v[62:63], 0\n\t"
      "v_mfma_f32_4x4x4_16b_bf16 v[148:151], v[134:135], v[64:65], 0\n\t"
      "v_mfma_f32_16x16x32_bf16 v[90:93], v[42:45], v[22:25], v[90:93]\n\t"
      "ds_read2_b32 v[130:131], v5 offset0:0 offset1:2\n\t"
      "v_mfma_f32_16x16x32_bf16 v[94:97], v[46:49], v[22:25], v[94:97]\n\t"
      "ds_read_b128 v[22:25], v2 offset:6144\n\t"
      "v_mfma_f32_16x16x32_bf16 v[98:101], v[42:45], v[26:29], v[98:101]\n\t"
      "v_cvt_pk_bf16_f32 v50, v136, v137\n\t"
      "v_cvt_pk_bf16_f32 v51, v138, v139\n\t"
      "v_mfma_f32_16x16x32_bf16 v[102:105], v[46:49], v[26:29], v[102:105]\n\t"
      "ds_read_b128 v[26:29], v2 offset:8192\n\t"
      "v_mfma_f32_16x16x32_bf16 v[106:109], v[42:45], v[30:33], v[106:109]\n\t"
      "v_cvt_pk_bf16_f32 v52, v140, v141\n\t"
      "v_cvt_pk_bf16_f32 v53, v142, v143\n\t"
      "v_mfma_f32_16x16x32_bf16 v[110:113], v[46:49], v[30:33], v[110:113]\n\t"
      "ds_read_b128 v[30:33], v2 offset:10240\n\t"
      "v_mfma_f32_16x16x32_bf16 v[114:117], v[42:45], v[34:37], v[114:117]\n\t"
      "v_cvt_pk_bf16_f32 v54, v144, v145\n\t"
      "v_cvt_pk_bf16_f32 v55, v146, v147\n\t"
      "v_mfma_f32_16x16x32_bf16 v[118:121], v[46:49], v[34:37], v[118:121]\n\t"
      "ds_read_b128 v[34:37], v2 offset:12288\n\t"
      "v_mfma_f32_16x16x32_bf16 v[122:125], v[42:45], v[38:41], v[122:125]\n\t"
      "v_cvt_pk_bf16_f32 v56, v148, v149\n\t"
      "v_cvt_pk_bf16_f32 v57, v150, v151\n\t"
      "v_mfma_f32_16x16x32_bf16 v[126:129], v[46:49], v[38:41], v[126:129]\n\t"
      "ds_read_b128 v[38:41], v2 offset:14336\n\t"
      "s_barrier\n\t"
      "s_waitcnt lgkmcnt(5)\n\t"
      "v_mfma_f32_16x16x32_bf16 v[66:69], v[50:53], v[10:13], v[66:69]\n\t"
      "ds_read_b64_tr_b16 v[58:59], v3 offset:32768\n\t"
      "ds_read_b64_tr_b16 v[60:61], v3 offset:33792\n\t"
      "v_mfma_f32_16x16x32_bf16 v[70:73], v[54:57], v[10:13], v[70:73]\n\t"
      "ds_read_b64_tr_b16 v[62:63], v4 offset:32768\n\t"
      "ds_read_b64_tr_b16 v[64:65], v4 offset:33792\n\t"
      "v_mfma_f32_16x16x32_bf16 v[74:77], v[50:53], v[14:17], v[74:77]\n\t"
      "v_and_b32 v132, v7, v130\n\t"
      "v_and_b32 v133, v8, v130\n\t"
      "v_mfma_f32_16x16x32_bf16 v[78:81], v[54:57], v[14:17], v[78:81]\n\t"
      "ds_read_b128 v[10:13], v1 offset:32768\n\t"
      "v_and_b32 v134, v7, v131\n\t"
      "v_mfma_f32_16x16x32_bf16 v[82:85], v[50:53], v[18:21], v[82:85]\n\t"
      "ds_read_b128 v[14:17], v1 offset:34816\n\t"
      "v_and_b32 v135, v8, v131\n\t"
      "v_mfma_f32_16x16x32_bf16 v[86:89], v[54:57], v[18:21], v[86:89]\n\t"
      "ds_read_b128 v[18:21], v1 offset:36864\n\t"
      "s_waitcnt lgkmcnt(3)\n\t"
      "v_mfma_f32_4x4x4_16b_bf16 v[136:139], v[132:133], v[58:59], 0\n\t"
      "v_mfma_f32_4x4x4_16b_bf16 v[140:143], v[134:135], v[60:61], 0\n\t"
      "v_mfma_f32_4x4x4_16b_bf16 v[144:147], v[132:133], v[62:63], 0\n\t"
      "v_mfma_f32_4x4x4_16b_bf16 v[148:151], v[134:135], v[64:65], 0\n\t"
      "v_mfma_f32_16x16x32_bf16 v[90:93], v[50:53], v[22:25], v[90:93]\n\t"
      "ds_read2_b32 v[130:131], v5 offset0:16 offset1:18\n\t"
      "v_mfma_f32_16x16x32_bf16 v[94:97], v[54:57], v[22:25], v[94:97]\n\t"
      "ds_read_b128 v[22:25], v1 offset:38912\n\t"
      "v_add_u32 v5, 0x80, v5\n\t"
      "v_mfma_f32_16x16x32_bf16 v[98:101], v[50:53], v[26:29], v[98:101]\n\t"
      "v_cvt_pk_bf16_f32 v42, v136, v137\n\t"
      "v_cvt_pk_bf16_f32 v43, v138, v139\n\t"
      "v_mfma_f32_16x16x32_bf16 v[102:105], v[54:57], v[26:29], v[102:105]\n\t"
      "ds_read_b128 v[26:29], v1 offset:40960\n\t"
      "v_bfi_b32 v5, s23, v5, v6\n\t"
      "v_mfma_f32_16x16x32_bf16 v[106:109], v[50:53], v[30:33], v[106:109]\n\t"
      "v_cvt_pk_bf16_f32 v44, v140, v141\n\t"
      "v_cvt_pk_bf16_f32 v45, v142, v143\n\t"
      "v_mfma_f32_16x16x32_bf16 v[110:113], v[54:57], v[30:33], v[110:113]\n\t"
      "ds_read_b128 v[30:33], v1 offset:43008\n\t"
      "v_mfma_f32_16x16x32_bf16 v[114:117], v[50:53], v[34:37], v[114:117]\n\t"
      "v_cvt_pk_bf16_f32 v46, v144, v145\n\t"
      "v_cvt_pk_bf16_f32 v47, v146, v147\n\t"
      "v_mfma_f32_16x16x32_bf16 v[118:121], v[54:57], v[34:37], v[118:121]\n\t"
      "ds_read_b128 v[34:37], v1 offset:45056\n\t"
      "v_mfma_f32_16x16x32_bf16 v[122:125], v[50:53], v[38:41], v[122:125]\n\t"
      "v_cvt_pk_bf16_f32 v48, v148, v149\n\t"
      "v_cvt_pk_bf16_f32 v49, v150, v151\n\t"
      "v_mfma_f32_16x16x32_bf16 v[126:129], v[54:57], v[38:41], v[126:129]\n\t"
      "ds_read_b128 v[38:41], v1 offset:47104\n\t"
      "s_waitcnt lgkmcnt(5)\n\t"
      "v_mfma_f32_16x16x32_bf16 v[66:69], v[42:45], v[10:13], v[66:69]\n\t"
      "s_sub_u32 s20, s20, 1\n\t"
      "ds_read_b64_tr_b16 v[58:59], v3 offset:40960\n\t"
      "ds_read_b64_tr_b16 v[60:61], v3 offset:41984\n\t"
      "v_mfma_f32_16x16x32_bf16 v[70:73], v[46:49], v[10:13], v[70:73]\n\t"
      "ds_read_b64_tr_b16 v[62:63], v4 offset:40960\n\t"
      "ds_read_b64_tr_b16 v[64:65], v4 offset:41984\n\t"
      "v_mfma_f32_16x16x32_bf16 v[74:77], v[42:45], v[14:17], v[74:77]\n\t"
      "v_and_b32 v132, v7, v130\n\t"
      "v_and_b32 v133, v8, v130\n\t"
      "v_mfma_f32_16x16x32_bf16 v[78:81], v[46:49], v[14:17], v[78:81]\n\t"
      "ds_read_b128 v[10:13], v2 offset:32768\n\t"
      "v_and_b32 v134, v7, v131\n\t"
      "v_mfma_f32_16x16x32_bf16 v[82:85], v[42:45], v[18:21], v[82:85]\n\t"
      "ds_read_b128 v[14:17], v2 offset:34816\n\t"
      "v_and_b32 v135, v8, v131\n\t"
      "v_mfma_f32_16x16x32_bf16 v[86:89], v[46:49], v[18:21], v[86:89]\n\t"
      "ds_read_b128 v[18:21], v2 offset:36864\n\t"
      "s_waitcnt lgkmcnt(3)\n\t"
      "v_mfma_f32_4x4x4_16b_bf16 v[136:139], v[132:133], v[58:59], 0\n\t"
      "v_mfma_f32_4x4x4_16b_bf16 v[140:143], v[134:135], v[60:61], 0\n\t"
      "v_mfma_f32_4x4x4_16b_bf16 v[144:147], v[132:133], v[62:63], 0\n\t"
      "v_mfma_f32_4x4x4_16b_bf16 v[148:151], v[134:135], v[64:65], 0\n\t"
      "v_mfma_f32_16x16x32_bf16 v[90:93], v[42:45], v[22:25], v[90:93]\n\t"
      "ds_read2_b32 v[130:131], v5 offset0:0 offset1:2\n\t"
      "v_mfma_f32_16x16x32_bf16 v[94:97], v[46:49], v[22:25], v[94:97]\n\t"
      "ds_read_b128 v[22:25], v2 offset:38912\n\t"
      "v_xor_b32 v3, s22, v3\n\t"
      "v_mfma_f32_16x16x32_bf16 v[98:101], v[42:45], v[26:29], v[98:101]\n\t"
      "v_cvt_pk_bf16_f32 v50, v136, v137\n\t"
      "v_cvt_pk_bf16_f32 v51, v138, v139\n\t"
      "v_mfma_f32_16x16x32_bf16 v[102:105], v[46:49], v[26:29], v[102:105]\n\t"
      "ds_read_b128 v[26:29], v2 offset:40960\n\t"
      "v_xor_b32 v4, s22, v4\n\t"
      "v_mfma_f32_16x16x32_bf16 v[106:109], v[42:45], v[30:33], v[106:109]\n\t"
      "v_cvt_pk_bf16_f32 v52, v140, v141\n\t"
      "v_cvt_pk_bf16_f32 v53, v142, v143\n\t"
      "v_mfma_f32_16x16x32_bf16 v[110:113], v[46:49], v[30:33], v[110:113]\n\t"
      "ds_read_b128 v[30:33], v2 offset:43008\n\t"
      "v_xor_b32 v1, s22, v1\n\t"
      "v_mfma_f32_16x16x32_bf16 v[114:117], v[42:45], v[34:37], v[114:117]\n\t"
      "v_cvt_pk_bf16_f32 v54, v144, v145\n\t"
      "v_cvt_pk_bf16_f32 v55, v146, v147\n\t"
      "v_mfma_f32_16x16x32_bf16 v[118:121], v[46:49], v[34:37], v[118:121]\n\t"
      "ds_read_b128 v[34:37], v2 offset:45056\n\t"
      "v_mfma_f32_16x16x32_bf16 v[122:125], v[42:45], v[38:41], v[122:125]\n\t"
      "v_cvt_pk_bf16_f32 v56, v148, v149\n\t"
      "v_cvt_pk_bf16_f32 v57, v150, v151\n\t"
      "v_mfma_f32_16x16x32_bf16 v[126:129], v[46:49], v[38:41], v[126:129]\n\t"
      "ds_read_b128 v[38:41], v2 offset:47104\n\t"
      "s_barrier\n\t"
      "s_waitcnt lgkmcnt(5)\n\t"
      "v_mfma_f32_16x16x32_bf16 v[66:69], v[50:53], v[10:13], v[66:69]\n\t"
      "ds_read_b64_tr_b16 v[58:59], v3 offset:0\n\t"
      "ds_read_b64_tr_b16 v[60:61], v3 offset:1024\n\t"
      "v_mfma_f32_16x16x32_bf16 v[70:73], v[54:57], v[10:13], v[70:73]\n\t"
      "ds_read_b64_tr_b16 v[62:63], v4 offset:0\n\t"
      "ds_read_b64_tr_b16 v[64:65], v4 offset:1024\n\t"
      "v_mfma_f32_16x16x32_bf16 v[74:77], v[50:53], v[14:17], v[74:77]\n\t"
      "v_and_b32 v132, v7, v130\n\t"
      "v_and_b32 v133, v8, v130\n\t"
      "v_mfma_f32_16x16x32_bf16 v[78:81], v[54:57], v[14:17], v[78:81]\n\t"
      "ds_read_b128 v[10:13], v1 offset:0\n\t"
      "v_and_b32 v134, v7, v131\n\t"
      "v_mfma_f32_16x16x32_bf16 v[82:85], v[50:53], v[18:21], v[82:85]\n\t"
      "ds_read_b128 v[14:17], v1 offset:2048\n\t"
      "v_and_b32 v135, v8, v131\n\t"
      "v_mfma_f32_16x16x32_bf16 v[86:89], v[54:57], v[18:21], v[86:89]\n\t"
      "ds_read_b128 v[18:21], v1 offset:4096\n\t"
      "s_waitcnt lgkmcnt(3)\n\t"
      "v_mfma_f32_4x4x4_16b_bf16 v[136:139], v[132:133], v[58:59], 0\n\t"
      "v_mfma_f32_4x4x4_16b_bf16 v[140:143], v[134:135], v[60:61], 0\n\t"
      "v_mfma_f32_4x4x4_16b_bf16 v[144:147], v[132:133], v[62:63], 0\n\t"
      "v_mfma_f32_4x4x4_16b_bf16 v[148:151], v[134:135], v[64:65], 0\n\t"
      "v_mfma_f32_16x16x32_bf16 v[90:93], v[50:53], v[22:25], v[90:93]\n\t"
      "ds_read2_b32 v[130:131], v5 offset0:16 offset1:18\n\t"
      "v_mfma_f32_16x16x32_bf16 v[94:97], v[54:57], v[22:25], v[94:97]\n\t"
      "ds_read_b128 v[22:25], v1 offset:6144\n\t"
      "v_add_u32 v5, 0x80, v5\n\t"
      "v_mfma_f32_16x16x32_bf16 v[98:101], v[50:53], v[26:29], v[98:101]\n\t"
      "v_cvt_pk_bf16_f32 v42, v136, v137\n\t"
      "v_cvt_pk_bf16_f32 v43, v138, v139\n\t"
      "v_mfma_f32_16x16x32_bf16 v[102:105], v[54:57], v[26:29], v[102:105]\n\t"
      "ds_read_b128 v[26:29], v1 offset:8192\n\t"
      "v_bfi_b32 v5, s23, v5, v6\n\t"
      "v_mfma_f32_16x16x32_bf16 v[106:109], v[50:53], v[30:33], v[106:109]\n\t"
      "v_cvt_pk_bf16_f32 v44, v140, v141\n\t"
      "v_cvt_pk_bf16_f32 v45, v142, v143\n\t"
      "v_mfma_f32_16x16x32_bf16 v[110:113], v[54:57], v[30:33], v[110:113]\n\t"
      "ds_read_b128 v[30:33], v1 offset:10240\n\t"
      "v_xor_b32 v2, s22, v2\n\t"
      "v_mfma_f32_16x16x32_bf16 v[114:117], v[50:53], v[34:37], v[114:117]\n\t"
      "v_cvt_pk_bf16_f32 v46, v144, v145\n\t"
      "v_cvt_pk_bf16_f32 v47, v146, v147\n\t"
      "v_mfma_f32_16x16x32_bf16 v[118:121], v[54:57], v[34:37], v[118:121]\n\t"
      "ds_read_b128 v[34:37], v1 offset:12288\n\t"
      "v_mfma_f32_16x16x32_bf16 v[122:125], v[50:53], v[38:41], v[122:125]\n\t"
      "v_cvt_pk_bf16_f32 v48, v148, v149\n\t"
      "s_cmp_eq_u32 s20, 0\n\t"
      "v_cvt_pk_bf16_f32 v49, v150, v151\n\t"
      "v_mfma_f32_16x16x32_bf16 v[126:129], v[54:57], v[38:41], v[126:129]\n\t"
      "ds_read_b128 v[38:41], v1 offset:14336\n\t"
      "s_cbranch_scc1 3f\n\t"
      "2:\n\t"
      "s_waitcnt lgkmcnt(5)\n\t"
      "v_mfma_f32_16x16x32_bf16 v[66:69], v[42:45], v[10:13], v[66:69]\n\t"
      "ds_read_b64_tr_b16 v[58:59], v3 offset:8192\n\t"
      "ds_read_b64_tr_b16 v[60:61], v3 offset:9216\n\t"
      "v_mfma_f32_16x16x32_bf16 v[70:73], v[46:49], v[10:13], v[70:73]\n\t"
      "ds_read_b64_tr_b16 v[62:63], v4 offset:8192\n\t"
      "ds_read_b64_tr_b16 v[64:65], v4 offset:9216\n\t"
      "v_mfma_f32_16x16x32_bf16 v[74:77], v[42:45], v[14:17], v[74:77]\n\t"
      "v_and_b32 v132, v7, v130\n\t"
      "v_and_b32 v133, v8, v130\n\t"
      "v_mfma_f32_16x16x32_bf16 v[78:81], v[46:49], v[14:17], v[78:81]\n\t"
      "ds_read_b128 v[10:13], v2 offset:0\n\t"
      "v_and_b32 v134, v7, v131\n\t"
      "v_mfma_f32_16x16x32_bf16 v[82:85], v[42:45], v[18:21], v[82:85]\n\t"
      "ds_read_b128 v[14:17], v2 offset:2048\n\t"
      "v_and_b32 v135, v8, v131\n\t"
      "v_mfma_f32_16x16x32_bf16 v[86:89], v[46:49], v[18:21], v[86:89]\n\t"
      "ds_read_b128 v[18:21], v2 offset:4096\n\t"
      "s_waitcnt lgkmcnt(3)\n\t"
      "v_mfma_f32_4x4x4_16b_bf16 v[136:139], v[132:133], v[58:59], 0\n\t"
      "v_mfma_f32_4x4x4_16b_bf16 v[140:143], v[134:135], v[60:61], 0\n\t"
      "v_mfma_f32_4x4x4_16b_bf16 v[144:147], v[132:133], v[62:63], 0\n\t"
      "v_mfma_f32_4x4x4_16b_bf16 v[148:151], v[134:135], v[64:65], 0\n\t"
      "v_mfma_f32_16x16x32_bf16 v[90:93], v[42:45], v[22:25], v[90:93]\n\t"
      "ds_read2_b32 v[130:131], v5 offset0:0 offset1:2\n\t"
      "v_mfma_f32_16x16x32_bf16 v[94:97], v[46:49], v[22:25], v[94:97]\n\t"
      "ds_read_b128 v[22:25], v2 offset:6144\n\t"
      "v_mfma_f32_16x16x32_bf16 v[98:101], v[42:45], v[26:29], v[98:101]\n\t"
      "v_cvt_pk_bf16_f32 v50, v136, v137\n\t"
      "v_cvt_pk_bf16_f32 v51, v138, v139\n\t"
      "v_mfma_f32_16x16x32_bf16 v[102:105], v[46:49], v[26:29], v[102:105]\n\t"
      "ds_read_b128 v[26:29], v2 offset:8192\n\t"
      "v_mfma_f32_16x16x32_bf16 v[106:109], v[42:45], v[30:33], v[106:109]\n\t"
      "v_cvt_pk_bf16_f32 v52, v140, v141\n\t"
      "v_cvt_pk_bf16_f32 v53, v142, v143\n\t"
      "v_mfma_f32_16x16x32_bf16 v[110:113], v[46:49], v[30:33], v[110:113]\n\t"
      "ds_read_b128 v[30:33], v2 offset:10240\n\t"
      "v_mfma_f32_16x16x32_bf16 v[114:117], v[42:45], v[34:37], v[114:117]\n\t"
      "v_cvt_pk_bf16_f32 v54, v144, v145\n\t"
      "v_cvt_pk_bf16_f32 v55, v146, v147\n\t"
      "v_mfma_f32_16x16x32_bf16 v[118:121], v[46:49], v[34:37], v[118:121]\n\t"
      "ds_read_b128 v[34:37], v2 offset:12288\n\t"
      "v_mfma_f32_16x16x32_bf16 v[122:125], v[42:45], v[38:41], v[122:125]\n\t"
      "v_cvt_pk_bf16_f32 v56, v148, v149\n\t"
      "v_cvt_pk_bf16_f32 v57, v150, v151\n\t"
      "v_mfma_f32_16x16x32_bf16 v[126:129], v[46:49], v[38:41], v[126:129]\n\t"
      "ds_read_b128 v[38:41], v2 offset:14336\n\t"
      "s_branch 1b\n\t"
      "3:\n\t"
      "s_waitcnt lgkmcnt(0)\n\t"
      "s_nop 7\n\t"
      : "={v[66:81]}"(acc0), "={v[82:97]}"(acc1), "={v[98:113]}"(acc2), "={v[114:129]}"(acc3),
        "+{v1}"(xa0), "+{v2}"(xa1), "+{v3}"(wa0), "+{v4}"(wa1), "+{v5}"(ga)
      : "{v7}"(ma), "{v8}"(mb)
      : "memory", "scc", "s20", "s22", "s23", "v6","v9","v10","v11","v12","v13","v14","v15","v16","v17","v18","v19","v20","v21","v22","v23","v24","v25","v26","v27","v28","v29","v30","v31","v32","v33","v34","v35","v36","v37","v38","v39","v40","v41","v42","v43","v44","v45","v46","v47","v48","v49","v50","v51","v52","v53","v54","v55","v56","v57","v58","v59","v60","v61","v62","v63","v64","v65","v130","v131","v132","v133","v134","v135","v136","v137","v138","v139","v140","v141","v142","v143","v144","v145","v146","v147","v148","v149","v150","v151");

    bar();
    f32x4 c1[2], c2[2];
    float2 st[8];
#pragma unroll
    for (int nt = 0; nt < 2; ++nt) {
      const int n0 = wq * 32 + nt * 16 + 4 * kg;
      c1[nt] = *(const f32x4*)(smem + OFF_C + n0 * 4);
      c2[nt] = *(const f32x4*)(smem + OFF_C + 512 + n0 * 4);
    }
#pragma unroll
    for (int mt = 0; mt < 8; ++mt) st[mt] = *(const float2*)(smem + OFF_ST + (mt * 16 + m16) * 8);
    char* ob = smem + OFF_OUT + m16 * 256 + (((wq * 4 + (kg & 1) * 2 + (kg >> 1)) ^ (lane & 7)) * 16);
    auto fin = [&](const f32x16& acc, int mp) {
#pragma unroll
      for (int mi = 0; mi < 2; ++mi) {
        const int mt = mp * 2 + mi;
        u32 d[2][2];
#pragma unroll
        for (int nt = 0; nt < 2; ++nt) {
          float o[4];
#pragma unroll
          for (int e = 0; e < 4; ++e) o[e] = acc[8 * mi + 4 * nt + e] * st[mt].x + (c1[nt][e] * st[mt].y + c2[nt][e]);
          d[nt][0] = cvt_pk(o[0], o[1]); d[nt][1] = cvt_pk(o[2], o[3]);
        }
        auto r0 = __builtin_amdgcn_permlane16_swap(d[0][0], d[1][0], false, false);
        auto r1 = __builtin_amdgcn_permlane16_swap(d[0][1], d[1][1], false, false);
        u32x4 v = {r0[0], r1[0], r0[1], r1[1]};
        *(u32x4*)(ob + mt * 4096) = v;
      }
    };
    fin(acc0, 0); fin(acc1, 1);
    wait_lgkm0(); bar();
    fin(acc2, 2); fin(acc3, 3);
    wait_lgkm0();
  } else {
    const int lw = wq;
    const int self = rp * 8 + cp;
    const __amdgpu_buffer_rsrc_t srsrc = __builtin_amdgcn_make_buffer_rsrc((void*)ws, 0, WS_BYTES, 0x00020000);
    int peer[3]; u32 ptag[3], tag;
    {
      u32 t0, r1, r2, r3, c1_, c2_, c3_;
      const char* gb = ws + self * 128;
      asm volatile("s_load_dword %0, %7, 0\n\ts_load_dword %1, %8, %9\n\ts_load_dword %2, %8, %10\n\ts_load_dword %3, %8, %11\n\t"
                   "s_load_dword %4, %8, %12\n\ts_load_dword %5, %8, %13\n\ts_load_dword %6, %8, %14\n\ts_waitcnt lgkmcnt(0)"
                   : "=&s"(t0), "=&s"(r1), "=&s"(r2), "=&s"(r3), "=&s"(c1_), "=&s"(c2_), "=&s"(c3_)
                   : "s"(gb), "s"(ws), "s"((self ^ 1) * 128), "s"((self ^ 2) * 128), "s"((self ^ 3) * 128),
                     "s"((self ^ 64) * 128), "s"((self ^ 128) * 128), "s"((self ^ 192) * 128) : "memory");
      tag = t0; ptag[0] = h ? c1_ : r1; ptag[1] = h ? c2_ : r2; ptag[2] = h ? c3_ : r3;
    }
#pragma unroll
    for (int k = 0; k < 3; ++k) peer[k] = h ? (self ^ ((k + 1) * 64)) : (self ^ (k + 1));
    const u32 slot = WS_MB + h * 2048 + (lw * 32 + (lane & 31)) * 16;
    const u32 oa = h ? OFF_C + (lw * 32 + (lane & 31)) * 4 : OFF_ST + (lw * 32 + lane) * 8, ob = oa + (h ? 512 : 4);
    const u32 seladdr = OFF_B16 + h * 16, gaddr = OFF_G16 + h * 16 + (p4 & 2) * 2;
    const u32 hm = (p4 & 1) ? 0xffff0000u : 0xffffu, ma = (p4 & 2) ? 0u : hm, mb = (p4 & 2) ? hm : 0u;
    const s16x4 ones = {0x3f80, 0x3f80, 0x3f80, 0x3f80};
    const f32x4 z4 = {0.f, 0.f, 0.f, 0.f};
    f32x4 hb0 = z4, hb1 = z4, hg0 = z4, hg1 = z4, gr0 = z4, gr1 = z4, sx0 = z4, sx1 = z4;
    const u32 xrow = (lw * 32 + (lane & 31)) * 128;
    auto extras = [&](int u) {
      const int kt = (u + rot) & 15;
      const char* sl = smem + (u & (NS - 1)) * SLOT;
      bf16x8 xf[4], sf[4];
      s16x4 wl[4], wh[4], dl[4], dh[4];
#pragma unroll
      for (int s = 0; s < 4; ++s) {
        const u32 ga = *(const u32*)(smem + gaddr + kt * 128 + s * 32), gc = *(const u32*)(smem + gaddr + kt * 128 + s * 32 + 8);
        dl[s] = __builtin_bit_cast(s16x4, (u32x2){ga & ma, ga & mb}); dh[s] = __builtin_bit_cast(s16x4, (u32x2){gc & ma, gc & mb});
        wl[s] = __builtin_amdgcn_ds_read_tr16_b64_v4i16((lds_s16x4_p)(sl + waddr + s * 4096));
        wh[s] = __builtin_amdgcn_ds_read_tr16_b64_v4i16((lds_s16x4_p)(sl + waddr + s * 4096 + 1024));
        xf[s] = *(const bf16x8*)(sl + xrow + (((2 * s + h) ^ xr) * 16));
        sf[s] = *(const bf16x8*)(smem + seladdr + kt * 128 + s * 32);
      }
      wait_lgkm0();
      __builtin_amdgcn_sched_barrier(0);
      __builtin_amdgcn_s_setprio(3);
      f32x4 pl[4], ph[4];
#pragma unroll
      for (int s = 0; s < 4; ++s) {
        pl[s] = __builtin_amdgcn_mfma_f32_4x4x4bf16_1k(dl[s], wl[s], z4, 0, 0, 0);
        ph[s] = __builtin_amdgcn_mfma_f32_4x4x4bf16_1k(dh[s], wh[s], z4, 0, 0, 0);
      }
#pragma unroll
      for (int s = 0; s < 4; ++s) {
        gr0 = __builtin_amdgcn_mfma_f32_4x4x4bf16_1k(lo4(xf[s]), lo4(xf[s]), gr0, 0, 0, 0);
        gr1 = __builtin_amdgcn_mfma_f32_4x4x4bf16_1k(hi4(xf[s]), hi4(xf[s]), gr1, 0, 0, 0);
        sx0 = __builtin_amdgcn_mfma_f32_4x4x4bf16_1k(ones, lo4(xf[s]), sx0, 0, 0, 0);
        sx1 = __builtin_amdgcn_mfma_f32_4x4x4bf16_1k(ones, hi4(xf[s]), sx1, 0, 0, 0);
        hb0 = __builtin_amdgcn_mfma_f32_4x4x4bf16_1k(lo4(sf[s]), wl[s], hb0, 0, 0, 0);
        hb1 = __builtin_amdgcn_mfma_f32_4x4x4bf16_1k(hi4(sf[s]), wh[s], hb1, 0, 0, 0);
      }
#pragma unroll
      for (int s = 0; s < 4; ++s) {
        const s16x4 rl = __builtin_bit_cast(s16x4, (u32x2){cvt_pk(pl[s][0], pl[s][1]), cvt_pk(pl[s][2], pl[s][3])});
        const s16x4 rh = __builtin_bit_cast(s16x4, (u32x2){cvt_pk(ph[s][0], ph[s][1]), cvt_pk(ph[s][2], ph[s][3])});
        hg0 = __builtin_amdgcn_mfma_f32_4x4x4bf16_1k(ones, rl, hg0, 0, 0, 0);
        hg1 = __builtin_amdgcn_mfma_f32_4x4x4bf16_1k(ones, rh, hg1, 0, 0, 0);
      }
      __builtin_amdgcn_s_setprio(0);
    };
    bar();
    int u = 0;
#pragma unroll 1
    for (; u < 4; ++u) { extras(u); bar(); }
    float va, vb;
    u32x4 gnl;
    {
      const f32x4 gs = gr0 + gr1;
      float sq = p4 == 0 ? gs[0] : p4 == 1 ? gs[1] : p4 == 2 ? gs[2] : gs[3];
      asm volatile("" : "+v"(sq));
      bar();
      const auto ra = __builtin_amdgcn_permlane32_swap(__float_as_uint(sx0[0] + sx1[0]), __float_as_uint(hg0[0] + hg1[0]), false, false);
      const auto rb = __builtin_amdgcn_permlane32_swap(__float_as_uint(sq), __float_as_uint(hb0[0] + hb1[0]), false, false);
      va = __uint_as_float(ra[0]) + __uint_as_float(ra[1]);
      vb = __uint_as_float(rb[0]) + __uint_as_float(rb[1]);
      asm volatile("" : "+v"(va), "+v"(vb));
      bar();
      tag += 1u; ptag[0] += 1u; ptag[1] += 1u; ptag[2] += 1u;
      gnl[0] = __float_as_uint(va); gnl[1] = tag; gnl[2] = __float_as_uint(vb); gnl[3] = tag;
      asm volatile("" : "+v"(gnl), "+v"(ptag[0]), "+v"(ptag[1]), "+v"(ptag[2]));
      u += 2;
    }
#pragma unroll 1
    for (; u < PUBU; ++u) bar();
    __builtin_amdgcn_raw_buffer_store_b128(gnl, srsrc, self * 4096 + slot, 0, 16);
#pragma unroll 1
    for (; u < GATU; ++u) bar();
    u32x4 in[3];
#pragma unroll
    for (int k = 0; k < 3; ++k) in[k] = __builtin_amdgcn_raw_buffer_load_b128(srsrc, peer[k] * 4096 + slot, 0, 16);
#pragma unroll 1
    for (; u < 16; ++u) bar();
    auto check = [&]() {
      u32 bad = 0;
#pragma unroll
      for (int k = 0; k < 3; ++k) bad |= (in[k][1] ^ ptag[k]) | (in[k][3] ^ ptag[k]);
      return __all(bad == 0) != 0;
    };
    bool all_ok = check();
    if (__builtin_expect(!all_ok, 0)) {
      const unsigned long long t0 = __builtin_amdgcn_s_memrealtime();
      do {
        __builtin_amdgcn_s_sleep(4);
        asm volatile("" ::: "memory");
#pragma unroll
        for (int k = 0; k < 3; ++k) in[k] = __builtin_amdgcn_raw_buffer_load_b128(srsrc, peer[k] * 4096 + slot, 0, 16);
        all_ok = check();
      } while (!all_ok && __builtin_amdgcn_s_memrealtime() - t0 < 20000ull);
    }
    va = (va + __uint_as_float(in[0][0])) + (__uint_as_float(in[1][0]) + __uint_as_float(in[2][0]));
    vb = (vb + __uint_as_float(in[0][2])) + (__uint_as_float(in[1][2]) + __uint_as_float(in[2][2]));
    if (__builtin_expect(!all_ok, 0)) {
      va = 0.f; vb = 0.f;
      const int e = lw * 32 + (lane & 31);
      const u16* p = h ? (w + cp * 128 + e) : (x + (size_t)(rp * 128 + e) * KD);
      const int stp = h ? ND : 1;
#pragma unroll 1
      for (int k = 0; k < KD; ++k) {
        const float v = __uint_as_float((u32)p[(size_t)k * stp] << 16);
        const float gk = __uint_as_float((u32)g[k] << 16), bk = __uint_as_float((u32)b[k] << 16);
        va += h ? __uint_as_float(cvt_pk(0.f, v * gk) & 0xffff0000u) : v; vb = __builtin_fmaf(v, h ? bk : v, vb);
      }
    }
    {
      const float mu = va * (1.f / KD);
      const float rs = __builtin_amdgcn_rsqf(fmaxf(vb * (1.f / KD) - mu * mu, 0.f) + 1e-5f);
      *(float*)(smem + oa) = h ? va : rs;
      *(float*)(smem + ob) = h ? vb : -mu * rs;
    }
    wait_lgkm0();
    bar();
    if (tid == 512) __builtin_amdgcn_raw_buffer_store_b32(tag, srsrc, self * 128, 0, 16);
    bar();
    {
      const int r0 = lw * 16 + (lane >> 4), ch = lane & 15;
#pragma unroll
      for (int i = 0; i < 4; ++i) {
        const int r = r0 + 4 * i;
        const u32x4 v = *(const u32x4*)(smem + OFF_OUT + r * 256 + ((ch ^ (r & 7)) * 16));
        __builtin_amdgcn_raw_buffer_store_b128(v, orsrc, (u32)(((rp * 128 + r) * ND + cp * 128 + ch * 8) * 2), 0, STAUX);
      }
    }
  }
  bar();
  {
    const int r0 = 64 + (wid - 4) * 8 + (lane >> 4), ch = lane & 15;
#pragma unroll
    for (int i = 0; i < 2; ++i) {
      const int r = r0 + 4 * i;
      const u32x4 v = *(const u32x4*)(smem + OFF_OUT + r * 256 + ((ch ^ (r & 7)) * 16));
      __builtin_amdgcn_raw_buffer_store_b128(v, orsrc, (u32)(((rp * 128 + r) * ND + cp * 128 + ch * 8) * 2), 0, STAUX);
    }
  }
}

extern "C" void kernel_launch(void* const* d_in, const int* in_sizes, int n_in,
                              void* d_out, int out_size, void* d_ws, size_t ws_size, hipStream_t stream) {
  const u16* x = (const u16*)d_in[0]; const u16* g = (const u16*)d_in[1];
  const u16* b = (const u16*)d_in[2]; const u16* w = (const u16*)d_in[3];
  (void)hipFuncSetAttribute((const void*)lnlin_kernel, hipFuncAttributeMaxDynamicSharedMemorySize, LDS_BYTES);
  lnlin_kernel<<<256, 768, LDS_BYTES, stream>>>(x, w, g, b, (u16*)d_out, (char*)d_ws);
}
```
